```python
import torch

def _softmax(x, axis=-1):
    return torch.softmax(x, dim=axis)
def _gelu(x):
    return torch.nn.functional.gelu(x, approximate="tanh")
def _silu(x):
    return torch.nn.functional.silu(x)
def _sigmoid(x):
    return torch.sigmoid(x)
def _layernorm(x, axis=-1, eps=1e-5):
    m = x.mean(dim=axis, keepdim=True)
    v = x.var(dim=axis, keepdim=True, unbiased=False)
    return (x - m) / torch.sqrt(v + eps)
def _scatter_add(tgt, idx, src, axis=0):
    return tgt.clone().index_add_(axis, idx, src)
def _segment_sum(src, seg, n):
    out = torch.zeros((n,) + src.shape[1:], dtype=src.dtype)
    return out.index_add_(0, seg, src)
def _pad(x, pw):
    flat = tuple(v for pair in reversed(pw) for v in pair)
    return torch.nn.functional.pad(x, flat)

kernel_name = 'standalone__adm_resblock__diffusion'

def setup_inputs():

    _w = (lambda a: (lambda t: t.to(torch.bfloat16) if t.is_floating_point() else t)(torch.as_tensor(a)))
    B, C, HW, E = 2, 512, 64, 768
    sc = (9.0 * C) ** -0.5
    return {
        "x": _w((lambda *s: torch.randn(*s, dtype=torch.bfloat16) * 0.05)(B, C, HW, HW)),
        "t_emb": _w((lambda *s: torch.randn(*s, dtype=torch.bfloat16) * 0.2)(B, E)),
        "g1": _w(1.0 + 0.1 * (lambda *s: torch.randn(*s, dtype=torch.bfloat16) * 0.2)(C)),
        "be1": _w(0.1 * (lambda *s: torch.randn(*s, dtype=torch.bfloat16) * 0.2)(C)),
        "w1": _w((lambda *s: torch.randn(*s, dtype=torch.bfloat16) * 0.2)(C, C, 3, 3) * sc),
        "w_emb": _w((lambda *s: torch.randn(*s, dtype=torch.bfloat16) * 0.2)(E, C) * (E ** -0.5)),
        "g2": _w(1.0 + 0.1 * (lambda *s: torch.randn(*s, dtype=torch.bfloat16) * 0.2)(C)),
        "be2": _w(0.1 * (lambda *s: torch.randn(*s, dtype=torch.bfloat16) * 0.2)(C)),
        "w2": _w((lambda *s: torch.randn(*s, dtype=torch.bfloat16) * 0.2)(C, C, 3, 3) * sc),
    }

def reference(x, t_emb, g1, be1, w1, w_emb, g2, be2, w2):
    B, C, H, W = x.shape
    G, eps = 4, 1e-5
    def gn(z, gamma, beta):
        zr = z.reshape(B, G, (C // G) * H * W)
        mu = zr.mean(-1).reshape(B, G, 1)
        var = ((zr - mu) ** 2).mean(-1).reshape(B, G, 1)
        zn = ((zr - mu) / torch.sqrt(var + eps)).reshape(B, C, H, W)
        return zn * gamma.reshape(1, C, 1, 1) + beta.reshape(1, C, 1, 1)
    def conv3(z, w):
        zp = _pad(z, ((0, 0), (0, 0), (1, 1), (1, 1)))
        taps = [torch.einsum("bchw,oc->bohw", zp[:, :, i:i+H, j:j+W], w[:, :, i, j]) for i in range(3) for j in range(3)]
        out = taps[0]
        for t in taps[1:]:
            out = out + t
        return out
    h = conv3(_silu(gn(x, g1, be1)), w1)
    emb = _silu(t_emb) @ w_emb
    h = h + emb.reshape(B, C, 1, 1)
    h = conv3(_silu(gn(h, g2, be2)), w2)
    return x + h
```

```cpp
#include <hip/hip_runtime.h>
#include <hip/hip_bf16.h>
#include <stdint.h>

typedef __attribute__((ext_vector_type(8))) short bf16x8;
typedef __attribute__((ext_vector_type(4))) float f32x4;
typedef __attribute__((ext_vector_type(4))) unsigned u32x4;
typedef __attribute__((ext_vector_type(2))) float f32x2;
typedef __attribute__((ext_vector_type(2))) __bf16 bf16x2_t;
#define GAS __attribute__((address_space(1)))
#define LAS __attribute__((address_space(3)))
typedef GAS unsigned long long gu64;
typedef GAS unsigned gu32;
#define RLX_AGENT __ATOMIC_RELAXED, __HIP_MEMORY_SCOPE_AGENT

constexpr int BN = 2, CH = 512, HH = 64, WW = 64, EE = 768;
constexpr float EPS = 1e-5f;

constexpr size_t ACT_BYTES = (size_t)BN * 66 * 64 * 512 * 2;
constexpr size_t WP_BYTES  = (size_t)512 * 512 * 9 * 2;
constexpr size_t OFF_A1 = 0;
constexpr size_t OFF_A2 = OFF_A1 + ACT_BYTES;
constexpr size_t OFF_W1 = OFF_A2 + ACT_BYTES;
constexpr size_t OFF_W2 = OFF_W1 + WP_BYTES;
constexpr size_t OFF_EMB = OFF_W2 + WP_BYTES;
constexpr size_t OFF_ST1 = OFF_EMB + 4096;
constexpr size_t OFF_SYNC = OFF_ST1 + 8 * 32 * 8;
constexpr size_t OFF_TMO = OFF_SYNC + 8 * 64 * 8;
constexpr size_t WS_END = OFF_TMO + 256;

constexpr int PITCH = 160;
constexpr int ROWB = 66 * PITCH;
constexpr int BUFB = 6 * ROWB;
constexpr int E1_PITCH = 272;
constexpr int E2_PITCH = 1040;
constexpr int LDS_SCR = 139264;
constexpr int LDS_BYTES = LDS_SCR + 1024;

__device__ __forceinline__ float bf2f(unsigned short v) { return __uint_as_float(((unsigned)v) << 16); }
__device__ __forceinline__ float bflo(unsigned v) { return __uint_as_float(v << 16); }
__device__ __forceinline__ float bfhi(unsigned v) { return __uint_as_float(v & 0xffff0000u); }
__device__ __forceinline__ unsigned pack2(float lo, float hi) {
    f32x2 v = {lo, hi}; bf16x2_t b = __builtin_convertvector(v, bf16x2_t); return __builtin_bit_cast(unsigned, b);
}
__device__ __forceinline__ float silu_f(float y) {
    return y * __builtin_amdgcn_rcpf(1.0f + __builtin_amdgcn_exp2f(-1.4426950408889634f * y));
}
__device__ __forceinline__ float wave_sum(float v) {
#pragma unroll
    for (int o = 32; o >= 1; o >>= 1) v += __shfl_xor(v, o, 64);
    return v;
}

__global__ __launch_bounds__(256) void k0_prep(const unsigned short* __restrict__ x, const unsigned short* __restrict__ temb,
                                               const unsigned short* __restrict__ w1, const unsigned short* __restrict__ wemb,
                                               const unsigned short* __restrict__ w2, char* __restrict__ ws) {
    __shared__ __attribute__((aligned(16))) char sm[16 * 1168 + 64];
    const int bid = blockIdx.x, t = threadIdx.x, lane = t & 63, wv = t >> 6;
    if (bid < 256) {
        const u32x4* p = (const u32x4*)(x + (size_t)bid * 16384);
        u32x4 v[8];
#pragma unroll
        for (int j = 0; j < 8; ++j) v[j] = p[j * 256 + t];
        float s = 0.f;
#pragma unroll
        for (int j = 0; j < 8; ++j)
#pragma unroll
            for (int e = 0; e < 4; ++e) s += bflo(v[j][e]) + bfhi(v[j][e]);
        s = wave_sum(s);
        const float mw = s * (1.0f / 4096.0f);
        float m2 = 0.f;
#pragma unroll
        for (int j = 0; j < 8; ++j)
#pragma unroll
            for (int e = 0; e < 4; ++e) { float a = bflo(v[j][e]) - mw, b = bfhi(v[j][e]) - mw; m2 += a * a + b * b; }
        m2 = wave_sum(m2);
        float* red = (float*)sm;
        if (lane == 0) { red[wv * 2] = mw; red[wv * 2 + 1] = m2; }
        __syncthreads();
        if (t == 0) {
            float mean = 0.25f * (red[0] + red[2] + red[4] + red[6]);
            float M2 = red[1] + red[3] + red[5] + red[7];
#pragma unroll
            for (int i = 0; i < 4; ++i) { float d = red[2 * i] - mean; M2 += 4096.0f * d * d; }
            float2* st = (float2*)(ws + OFF_ST1);
            st[bid] = make_float2(mean, M2);
        }
    } else if (bid < 768) {
        const int i = bid - 256, cv = i >> 8, og = (i & 255) >> 3, chunk = i & 7;
        const unsigned short* w = cv ? w2 : w1;
        unsigned short* wp = (unsigned short*)(ws + (cv ? OFF_W2 : OFF_W1));
        for (int q = t; q < 16 * 72; q += 256) {
            int r = q / 72, c16 = q % 72;
            u32x4 d = *(const u32x4*)((const char*)w + ((size_t)(og * 16 + r) * 512 + chunk * 64) * 18 + c16 * 16);
            *(u32x4*)(sm + r * 1168 + c16 * 16) = d;
        }
        __syncthreads();
        const int ntile = og >> 2, nf = og & 3;
        for (int q = t; q < 18 * 64; q += 256) {
            int fr = q >> 6, l = q & 63, tap = fr >> 1, kh = fr & 1;
            const unsigned short* src = (const unsigned short*)(sm + (l & 15) * 1168) + (kh * 32 + 8 * (l >> 4)) * 9 + tap;
            u32x4 d;
#pragma unroll
            for (int e = 0; e < 4; ++e) d[e] = (unsigned)src[(2 * e) * 9] | ((unsigned)src[(2 * e + 1) * 9] << 16);
            size_t o = ((((size_t)(ntile * 2 + kh) * 8 + chunk) * 9 + tap) * 4 + nf) * 512 + l * 8;
            *(u32x4*)(wp + o) = d;
        }
    } else if (bid < 784) {
        const int cb = (bid - 768) * 32;
        float* st = (float*)sm;
        for (int q = t; q < 2 * EE; q += 256) st[q] = silu_f(bf2f(temb[q]));
        __syncthreads();
        const int oc = t & 3, es = t >> 2;
        float a0[8], a1[8];
#pragma unroll
        for (int j = 0; j < 8; ++j) a0[j] = a1[j] = 0.f;
#pragma unroll
        for (int k = 0; k < 12; ++k) {
            int e = es + 64 * k;
            u32x4 d = *(const u32x4*)(wemb + (size_t)e * 512 + cb + oc * 8);
            float s0 = st[e], s1 = st[EE + e];
#pragma unroll
            for (int j = 0; j < 4; ++j) {
                a0[2 * j] += s0 * bflo(d[j]); a0[2 * j + 1] += s0 * bfhi(d[j]);
                a1[2 * j] += s1 * bflo(d[j]); a1[2 * j + 1] += s1 * bfhi(d[j]);
            }
        }
#pragma unroll
        for (int j = 0; j < 8; ++j)
#pragma unroll
            for (int o = 4; o < 64; o <<= 1) { a0[j] += __shfl_xor(a0[j], o, 64); a1[j] += __shfl_xor(a1[j], o, 64); }
        __syncthreads();
        float* red = (float*)sm;
        if (lane < 4) {
#pragma unroll
            for (int j = 0; j < 8; ++j) { red[wv * 64 + lane * 8 + j] = a0[j]; red[wv * 64 + 32 + lane * 8 + j] = a1[j]; }
        }
        __syncthreads();
        if (t < 64) {
            float s = red[t] + red[64 + t] + red[128 + t] + red[192 + t];
            float* emb = (float*)(ws + OFF_EMB);
            emb[(t >> 5) * 512 + cb + (t & 31)] = s;
        }
    } else {
        const int i = bid - 784;
        const int r = i >> 1, buf = r >> 2, b = (r >> 1) & 1, bot = r & 1;
        char* dst = ws + (buf ? OFF_A2 : OFF_A1) + ((size_t)b * 66 + (bot ? 65 : 0)) * 65536 + (i & 1) * 32768;
        u32x4 z = {0, 0, 0, 0};
#pragma unroll
        for (int j = 0; j < 8; ++j) *(u32x4*)(dst + (j * 256 + t) * 16) = z;
        if (i == 0) { *(u32x4*)(ws + OFF_SYNC + t * 16) = z; if (t < 16) *(u32x4*)(ws + OFF_TMO + t * 16) = z; }
    }
}

__device__ __forceinline__ void combine_stats(float mean_i, float m2_i, float n_i, float& mean, float& rstd) {
    float s = mean_i;
#pragma unroll
    for (int o = 16; o >= 1; o >>= 1) s += __shfl_xor(s, o, 64);
    mean = s * (1.0f / 32.0f);
    float d = mean_i - mean;
    float q = m2_i + n_i * d * d;
#pragma unroll
    for (int o = 16; o >= 1; o >>= 1) q += __shfl_xor(q, o, 64);
    float var = q / (32.0f * n_i);
    rstd = 1.0f / sqrtf(var + EPS);
}

__global__ __launch_bounds__(256) void k1_apply(const unsigned short* __restrict__ x, const unsigned short* __restrict__ g1,
                                                const unsigned short* __restrict__ be1, char* __restrict__ ws) {
    __shared__ unsigned sm[64 * 33];
    const int bid = blockIdx.x, t = threadIdx.x, lane = t & 63;
    const int chunk = bid & 7, y = (bid >> 3) & 63, b = bid >> 9;
    const int q = t >> 3, o = t & 7;
    const int c0 = chunk * 64 + 2 * q;
    const u32x4 v0 = *(const u32x4*)(x + (((size_t)b * 512 + c0) * 64 + y) * 64 + o * 8);
    const u32x4 v1 = *(const u32x4*)(x + (((size_t)b * 512 + c0 + 1) * 64 + y) * 64 + o * 8);
    const float2 st = ((const float2*)(ws + OFF_ST1))[(b * 4 + (chunk >> 1)) * 32 + (lane & 31)];
    const unsigned gg = *(const unsigned*)(g1 + c0), bb = *(const unsigned*)(be1 + c0);
    float mean, rstd;
    combine_stats(st.x, st.y, 16384.0f, mean, rstd);
    const float s0 = rstd * bflo(gg), s1 = rstd * bfhi(gg);
    const float t0 = bflo(bb) - mean * s0, t1 = bfhi(bb) - mean * s1;
#pragma unroll
    for (int e = 0; e < 4; ++e) {
        float a0 = silu_f(bflo(v0[e]) * s0 + t0), a1 = silu_f(bflo(v1[e]) * s1 + t1);
        float b0 = silu_f(bfhi(v0[e]) * s0 + t0), b1 = silu_f(bfhi(v1[e]) * s1 + t1);
        sm[(o * 8 + 2 * e) * 33 + q] = pack2(a0, a1);
        sm[(o * 8 + 2 * e + 1) * 33 + q] = pack2(b0, b1);
    }
    __syncthreads();
    char* a1 = ws + OFF_A1 + (((size_t)b * 66 + y + 1) * 64) * 1024 + chunk * 128;
#pragma unroll
    for (int j = 0; j < 2; ++j) {
        int px = (t >> 3) + 32 * j, pc = t & 7;
        const unsigned* s = sm + px * 33 + pc * 4;
        u32x4 d = {s[0], s[1], s[2], s[3]};
        *(u32x4*)(a1 + (size_t)px * 1024 + pc * 16) = d;
    }
}

template <bool CONV1>
__global__ __launch_bounds__(256, 1) void conv_kernel(const char* __restrict__ act, const char* __restrict__ wp,
                                                      const unsigned short* __restrict__ gam, const unsigned short* __restrict__ bet,
                                                      const unsigned short* __restrict__ xres, char* __restrict__ ws,
                                                      unsigned short* __restrict__ out) {
    extern __shared__ __attribute__((aligned(16))) char lds[];
    const int t = threadIdx.x, lane = t & 63;
    const int wv = __builtin_amdgcn_readfirstlane(t >> 6);
    const int mh = wv >> 1, kh = wv & 1;
    const int bid = blockIdx.x, xcd = bid & 7, idx = bid >> 3;
    const int ptile = (xcd >> 1) * 8 + (idx >> 2), ntile = (xcd & 1) * 4 + (idx & 3);
    const int b = ptile >> 4, y0 = (ptile & 15) * 4;

    if (t < 2 * 6 * 2 * 8) {
        int bu = t / 96, r = (t % 96) / 16, side = (t >> 3) & 1, pc = t & 7;
        *(u32x4*)(lds + bu * BUFB + r * ROWB + (side ? 65 * PITCH : 0) + pc * 16) = (u32x4){0, 0, 0, 0};
    }
    unsigned voff[5];
#pragma unroll
    for (int i = 0; i < 5; ++i) { int s = i * 64 + lane, px = s / 10, part = s % 10; if (part > 7) part = 7; voff[i] = px * 1024 + part * 16; }
    const char* abase = act + ((size_t)b * 66 + y0) * 65536;
    auto dma = [&](int chunk, int bu) {
#pragma unroll
        for (int u = 0; u < 3; ++u) {
            int hr = 3 * wv + u;
            const char* src = abase + (size_t)hr * 32768 + chunk * 128;
            char* dst = lds + bu * BUFB + (hr >> 1) * ROWB + (hr & 1) * 5120 + PITCH;
#pragma unroll
            for (int i = 0; i < 5; ++i)
                __builtin_amdgcn_global_load_lds((const GAS unsigned*)(src + voff[i]), (LAS unsigned*)(dst + i * 1024), 16, 0, 0);
        }
    };
    f32x4 acc[8][4];
#pragma unroll
    for (int m = 0; m < 8; ++m)
#pragma unroll
        for (int n = 0; n < 4; ++n) acc[m][n] = (f32x4){0.f, 0.f, 0.f, 0.f};

    const char* wst = wp + (size_t)(ntile * 2 + kh) * (72 * 4096) + lane * 16;
    const int abase_l = (mh * 2) * ROWB + (lane & 15) * PITCH + (lane >> 4) * 16 + kh * 64;
    dma(0, 0);
    for (int chunk = 0; chunk < 8; ++chunk) {
        asm volatile("s_waitcnt vmcnt(0)" ::: "memory");
        __syncthreads();
        if (chunk < 7) dma(chunk + 1, (chunk + 1) & 1);
        const char* lb = lds + (chunk & 1) * BUFB + abase_l;
#pragma unroll
        for (int tap = 0; tap < 9; ++tap) {
            const int dy = tap / 3, dx = tap % 3;
            bf16x8 wf[4], af[8];
#pragma unroll
            for (int n = 0; n < 4; ++n) wf[n] = *(const bf16x8*)(wst + (size_t)(chunk * 9 + tap) * 4096 + n * 1024);
#pragma unroll
            for (int m = 0; m < 8; ++m) af[m] = *(const bf16x8*)(lb + (dy + (m >> 2)) * ROWB + ((m & 3) * 16 + dx) * PITCH);
#pragma unroll
            for (int m = 0; m < 8; ++m)
#pragma unroll
                for (int n = 0; n < 4; ++n) {
                    if (CONV1) acc[m][n] = __builtin_amdgcn_mfma_f32_16x16x32_bf16(wf[n], af[m], acc[m][n], 0, 0, 0);
                    else       acc[m][n] = __builtin_amdgcn_mfma_f32_16x16x32_bf16(af[m], wf[n], acc[m][n], 0, 0, 0);
                }
        }
    }
    __syncthreads();
    if (CONV1) {
#pragma unroll
        for (int m = 0; m < 8; ++m)
#pragma unroll
            for (int n = 0; n < 4; ++n)
                *(f32x4*)(lds + kh * (256 * E1_PITCH) + (mh * 128 + m * 16 + (lane & 15)) * E1_PITCH + (n * 16 + 4 * (lane >> 4)) * 4) = acc[m][n];
        __syncthreads();
        const int oc = t & 7, pr = t >> 3;
        const float* emb = (const float*)(ws + OFF_EMB) + b * 512 + ntile * 64 + oc * 8;
        float e[8];
#pragma unroll
        for (int j = 0; j < 8; ++j) e[j] = emb[j];
        float h[8][8];
        float s = 0.f;
#pragma unroll
        for (int i = 0; i < 8; ++i) {
            const char* p0 = lds + (pr + 32 * i) * E1_PITCH + oc * 32;
            f32x4 u0 = *(const f32x4*)p0, u1 = *(const f32x4*)(p0 + 16);
            f32x4 w0 = *(const f32x4*)(p0 + 256 * E1_PITCH), w1 = *(const f32x4*)(p0 + 256 * E1_PITCH + 16);
#pragma unroll
            for (int j = 0; j < 4; ++j) { h[i][j] = u0[j] + w0[j] + e[j]; h[i][4 + j] = u1[j] + w1[j] + e[4 + j]; }
#pragma unroll
            for (int j = 0; j < 8; ++j) s += h[i][j];
        }
        s = wave_sum(s);
        const float mw = s * (1.0f / 4096.0f);
        float m2 = 0.f;
#pragma unroll
        for (int i = 0; i < 8; ++i)
#pragma unroll
            for (int j = 0; j < 8; ++j) { float d = h[i][j] - mw; m2 += d * d; }
        m2 = wave_sum(m2);
        float* red = (float*)(lds + LDS_SCR);
        if (lane == 0) { red[wv * 2] = mw; red[wv * 2 + 1] = m2; }
        __syncthreads();
        const int grp = b * 4 + (ntile >> 1);
        if (wv == 0) {
            float mean = 0.25f * (red[0] + red[2] + red[4] + red[6]);
            float M2 = red[1] + red[3] + red[5] + red[7];
#pragma unroll
            for (int i = 0; i < 4; ++i) { float d = red[2 * i] - mean; M2 += 4096.0f * d * d; }
            gu64* gr = (gu64*)(ws + OFF_SYNC) + grp * 64;
            const int slot = (ptile & 15) * 2 + (ntile & 1);
            if (lane < 2) {
                unsigned val = __float_as_uint(lane ? M2 : mean);
                __hip_atomic_store(gr + lane * 32 + slot, (1ull << 32) | val, RLX_AGENT);
            }
            unsigned long long g;
            unsigned spins = 0;
            while (true) {
                g = __hip_atomic_load(gr + lane, RLX_AGENT);
                if (__all((g >> 32) == 1ull)) break;
                __builtin_amdgcn_s_sleep(1);
                if (++spins > (1u << 20)) { if (lane == 0) __hip_atomic_store((gu32*)(ws + OFF_TMO), 1u, RLX_AGENT); break; }
            }
            float val = __uint_as_float((unsigned)g);
            float mi = __shfl(val, lane & 31, 64), qi = __shfl(val, 32 + (lane & 31), 64);
            float gm, gr_;
            combine_stats(mi, qi, 16384.0f, gm, gr_);
            if (lane == 0) { red[8] = gm; red[9] = gr_; }
        }
        __syncthreads();
        const float mean = red[8], rstd = red[9];
        const int cb = ntile * 64 + oc * 8;
        const u32x4 gg = *(const u32x4*)(gam + cb), bb = *(const u32x4*)(bet + cb);
        float sc[8], sh[8];
#pragma unroll
        for (int j = 0; j < 4; ++j) {
            sc[2 * j] = rstd * bflo(gg[j]); sc[2 * j + 1] = rstd * bfhi(gg[j]);
            sh[2 * j] = bflo(bb[j]) - mean * sc[2 * j]; sh[2 * j + 1] = bfhi(bb[j]) - mean * sc[2 * j + 1];
        }
        char* a2 = ws + OFF_A2 + (((size_t)b * 66 + y0 + 1) * 64) * 1024 + ntile * 128 + oc * 16;
#pragma unroll
        for (int i = 0; i < 8; ++i) {
            u32x4 d;
#pragma unroll
            for (int j = 0; j < 4; ++j)
                d[j] = pack2(silu_f(h[i][2 * j] * sc[2 * j] + sh[2 * j]), silu_f(h[i][2 * j + 1] * sc[2 * j + 1] + sh[2 * j + 1]));
            *(u32x4*)(a2 + (size_t)(pr + 32 * i) * 1024) = d;
        }
    } else {
#pragma unroll
        for (int m = 0; m < 8; ++m)
#pragma unroll
            for (int n = 0; n < 4; ++n)
                *(f32x4*)(lds + kh * (64 * E2_PITCH) + (n * 16 + (lane & 15)) * E2_PITCH + (mh * 128 + m * 16 + 4 * (lane >> 4)) * 4) = acc[m][n];
        __syncthreads();
        const int po = t & 31, cr = t >> 5;
#pragma unroll
        for (int i = 0; i < 8; ++i) {
            const int ch = cr + 8 * i;
            const char* p0 = lds + ch * E2_PITCH + po * 32;
            f32x4 u0 = *(const f32x4*)p0, u1 = *(const f32x4*)(p0 + 16);
            f32x4 w0 = *(const f32x4*)(p0 + 64 * E2_PITCH), w1 = *(const f32x4*)(p0 + 64 * E2_PITCH + 16);
            const size_t go = (((size_t)b * 512 + ntile * 64 + ch) * 64 + y0) * 64 + po * 8;
            const u32x4 xr = *(const u32x4*)(xres + go);
            u32x4 d;
            d[0] = pack2(u0[0] + w0[0] + bflo(xr[0]), u0[1] + w0[1] + bfhi(xr[0]));
            d[1] = pack2(u0[2] + w0[2] + bflo(xr[1]), u0[3] + w0[3] + bfhi(xr[1]));
            d[2] = pack2(u1[0] + w1[0] + bflo(xr[2]), u1[1] + w1[1] + bfhi(xr[2]));
            d[3] = pack2(u1[2] + w1[2] + bflo(xr[3]), u1[3] + w1[3] + bfhi(xr[3]));
            *(u32x4*)(out + go) = d;
        }
    }
}

extern "C" void kernel_launch(void* const* d_in, const int* in_sizes, int n_in, void* d_out, int out_size, void* d_ws,
                              size_t ws_size, hipStream_t stream) {
    const unsigned short* x = (const unsigned short*)d_in[0];
    const unsigned short* temb = (const unsigned short*)d_in[1];
    const unsigned short* g1 = (const unsigned short*)d_in[2];
    const unsigned short* be1 = (const unsigned short*)d_in[3];
    const unsigned short* w1 = (const unsigned short*)d_in[4];
    const unsigned short* wemb = (const unsigned short*)d_in[5];
    const unsigned short* g2 = (const unsigned short*)d_in[6];
    const unsigned short* be2 = (const unsigned short*)d_in[7];
    const unsigned short* w2 = (const unsigned short*)d_in[8];
    char* ws = (char*)d_ws;
    (void)hipFuncSetAttribute((const void*)conv_kernel<true>, hipFuncAttributeMaxDynamicSharedMemorySize, LDS_BYTES);
    (void)hipFuncSetAttribute((const void*)conv_kernel<false>, hipFuncAttributeMaxDynamicSharedMemorySize, LDS_BYTES);
    k0_prep<<<800, 256, 0, stream>>>(x, temb, w1, wemb, w2, ws);
    k1_apply<<<1024, 256, 0, stream>>>(x, g1, be1, ws);
    conv_kernel<true><<<256, 256, LDS_BYTES, stream>>>(ws + OFF_A1, ws + OFF_W1, g2, be2, x, ws, nullptr);
    conv_kernel<false><<<256, 256, LDS_BYTES, stream>>>(ws + OFF_A2, ws + OFF_W2, nullptr, nullptr, x, ws, (unsigned short*)d_out);
}
```

```cpp
#include <hip/hip_runtime.h>
#include <hip/hip_bf16.h>
#include <stdint.h>

typedef __attribute__((ext_vector_type(4))) float f32x4;
typedef __attribute__((ext_vector_type(4))) unsigned u32x4;
typedef __attribute__((ext_vector_type(2))) unsigned u32x2;
typedef __attribute__((ext_vector_type(2))) float f32x2;
typedef __attribute__((ext_vector_type(4))) short s16x4;
typedef __attribute__((ext_vector_type(2))) __bf16 bf16x2_t;
#define GAS __attribute__((address_space(1)))
#define LAS __attribute__((address_space(3)))
typedef GAS unsigned long long gu64;
typedef GAS unsigned gu32;
#define RLX_AGENT __ATOMIC_RELAXED, __HIP_MEMORY_SCOPE_AGENT
#define DI __device__ __forceinline__
#define AUX_SC1 16
#define SB() __builtin_amdgcn_sched_barrier(0)

constexpr int EE = 768;
constexpr float EPS = 1e-5f;

constexpr unsigned V_BYTES = 2u * 32 * 4 * 65536;
constexpr unsigned U_BYTES = 8u * 4 * 48 * 4096;
constexpr unsigned OFF_V1 = 0;
constexpr unsigned OFF_V2 = OFF_V1 + V_BYTES;
constexpr unsigned OFF_U1 = OFF_V2 + V_BYTES;
constexpr unsigned OFF_U2 = OFF_U1 + U_BYTES;
constexpr unsigned OFF_HX = OFF_U2 + U_BYTES;
constexpr unsigned OFF_EMB = OFF_HX + 256u * 16384;
constexpr unsigned OFF_S1 = OFF_EMB + 131072;
constexpr unsigned OFF_S2 = OFF_S1 + 32768;
constexpr unsigned OFF_FV1 = OFF_S2 + 32768;
constexpr unsigned OFF_FV2 = OFF_FV1 + 32768;
constexpr unsigned OFF_FU1 = OFF_FV2 + 32768;
constexpr unsigned OFF_FU2 = OFF_FU1 + 32768;
constexpr unsigned OFF_FH = OFF_FU2 + 32768;
constexpr unsigned OFF_G = OFF_FH + 32768;
constexpr unsigned WS_USED = OFF_G + 256;

constexpr int ROWP = 66 * 64, SLOT = 2 * ROWP, WREG = 34816;
constexpr int E1_PITCH = 272, E2_BLK = 4160;
constexpr int LDS_SCR = 4 * WREG;
constexpr int LDS_W = LDS_SCR + 1024;
constexpr int LDS_BYTES = LDS_W + 2 * 9216;

DI float bflo(unsigned v) { return __uint_as_float(v << 16); }
DI float bfhi(unsigned v) { return __uint_as_float(v & 0xffff0000u); }
DI f32x2 bf2(unsigned v) { return (f32x2){bflo(v), bfhi(v)}; }
DI unsigned pack2(float lo, float hi) {
    f32x2 v = {lo, hi}; bf16x2_t b = __builtin_convertvector(v, bf16x2_t); return __builtin_bit_cast(unsigned, b);
}
DI float silu_f(float y) { return y * __builtin_amdgcn_rcpf(1.0f + __builtin_amdgcn_exp2f(-1.4426950408889634f * y)); }
DI f32x2 silu2(f32x2 x, f32x2 s, f32x2 t, f32x2 s2, f32x2 t2) {
    const f32x2 y = x * s + t, z = x * s2 + t2;
    const f32x2 d = (f32x2){__builtin_amdgcn_exp2f(z[0]), __builtin_amdgcn_exp2f(z[1])} + 1.0f;
    return y * (f32x2){__builtin_amdgcn_rcpf(d[0]), __builtin_amdgcn_rcpf(d[1])};
}
constexpr float NLOG2E = -1.4426950408889634f;
template <int CTRL, int RMASK>
DI float dpp_add(float v) {
    return v + __int_as_float(__builtin_amdgcn_update_dpp(0, __float_as_int(v), CTRL, RMASK, 0xf, true));
}
DI float wave_sum(float v) {
    v = dpp_add<0x111, 0xf>(v); v = dpp_add<0x112, 0xf>(v); v = dpp_add<0x114, 0xf>(v); v = dpp_add<0x118, 0xf>(v);
    v = dpp_add<0x142, 0xa>(v); v = dpp_add<0x143, 0xc>(v);
    return __int_as_float(__builtin_amdgcn_readlane(__float_as_int(v), 63));
}
DI float half_sum(float v) {
    v = dpp_add<0x111, 0xf>(v); v = dpp_add<0x112, 0xf>(v); v = dpp_add<0x114, 0xf>(v); v = dpp_add<0x118, 0xf>(v);
    v = dpp_add<0x142, 0xa>(v);
    return __int_as_float(__builtin_amdgcn_readlane(__float_as_int(v), 31));
}
DI void st16(__amdgpu_buffer_rsrc_t rs, unsigned off, u32x4 v) { __builtin_amdgcn_raw_buffer_store_b128(v, rs, (int)off, 0, AUX_SC1); }
DI u32x4 ld16(__amdgpu_buffer_rsrc_t rs, unsigned off) { return __builtin_amdgcn_raw_buffer_load_b128(rs, (int)off, 0, AUX_SC1); }
DI u32x4 poll16(__amdgpu_buffer_rsrc_t rs, unsigned off) { asm volatile("" ::: "memory"); return ld16(rs, off); }
DI unsigned ld4(const char* ws, unsigned off) { return __hip_atomic_load((gu32*)(ws + off), RLX_AGENT); }
DI void st4(char* ws, unsigned off, unsigned v) { __hip_atomic_store((gu32*)(ws + off), v, RLX_AGENT); }
DI void drain() { asm volatile("s_waitcnt vmcnt(0)" ::: "memory"); }
DI void lds_barrier() { asm volatile("s_waitcnt lgkmcnt(0)\n\ts_barrier" ::: "memory"); }


constexpr int BGDELAY = 127;
constexpr unsigned SPIN_CAP = 1u << 16;
DI int fresh_tid() { int v = threadIdx.x; asm volatile("" : "+v"(v)); return v; }

DI void wave_stats(const f32x2 (&h)[16], float* red, int wv, int lane) {
    f32x2 s2 = h[0];
#pragma unroll
    for (int k = 1; k < 16; ++k) s2 += h[k];
    const float mw = wave_sum(s2[0] + s2[1]) * (1.0f / 2048.0f);
    f32x2 q2 = {0.f, 0.f};
#pragma unroll
    for (int k = 0; k < 16; ++k) { const f32x2 d = h[k] - mw; q2 += d * d; }
    const float m2 = wave_sum(q2[0] + q2[1]);
    if (lane == 0) { red[wv * 2] = mw; red[wv * 2 + 1] = m2; }
}
DI void stats_publish(__amdgpu_buffer_rsrc_t rs, unsigned off, int grp, int slot, unsigned tag, const float* red, int lane) {
    float mean = 0.f, M2 = 0.f;
#pragma unroll
    for (int i = 0; i < 8; ++i) { mean += red[2 * i]; M2 += red[2 * i + 1]; }
    mean *= 0.125f;
#pragma unroll
    for (int i = 0; i < 8; ++i) { const float d = red[2 * i] - mean; M2 += 2048.0f * d * d; }
    if (lane == 0) st16(rs, off + (unsigned)((grp * 32 + slot) * 128), (u32x4){__float_as_uint(mean), tag, __float_as_uint(M2), tag});
}
DI void stats_wait(__amdgpu_buffer_rsrc_t rs, unsigned off, int grp, unsigned tag, float* red, int lane) {
    const unsigned o = off + (unsigned)((grp * 32 + (lane & 31)) * 128);
    u32x4 g = poll16(rs, o);
    for (unsigned spins = 0; !__all(g[1] == tag && g[3] == tag);) {
        if (++spins > SPIN_CAP) break;
        __builtin_amdgcn_s_sleep(1);
        g = poll16(rs, o);
    }
    const float mi = __uint_as_float(g[0]);
    const float mean = half_sum(mi) * (1.0f / 32.0f);
    const float d = mi - mean;
    const float q = half_sum(__uint_as_float(g[2]) + 16384.0f * d * d);
    if (lane == 0) { red[16] = mean; red[17] = 1.0f / sqrtf(q * (1.0f / (32.0f * 16384.0f)) + EPS); }
}
DI bool last_arrival(float* red, int idx, int lane) {
    unsigned old = 0;
    if (lane == 0) old = __hip_atomic_fetch_add((LAS unsigned*)(red + idx), 1u, __ATOMIC_RELAXED, __HIP_MEMORY_SCOPE_WORKGROUP);
    return __builtin_amdgcn_readfirstlane(old) == 7u;
}
DI void uprep(const u32x4 (&rw)[9], u32x4 (&o)[12]) {
    unsigned r[36];
#pragma unroll
    for (int k = 0; k < 9; ++k) { r[4 * k] = rw[k][0]; r[4 * k + 1] = rw[k][1]; r[4 * k + 2] = rw[k][2]; r[4 * k + 3] = rw[k][3]; }
#pragma unroll
    for (int dx = 0; dx < 3; ++dx) {
        float g0[8], g2[8], u1[8], u2[8];
#pragma unroll
        for (int c = 0; c < 8; ++c) {
            const int i0 = c * 9 + dx, i1 = i0 + 3, i2 = i0 + 6;
            g0[c] = (i0 & 1) ? bfhi(r[i0 >> 1]) : bflo(r[i0 >> 1]);
            const float g1 = (i1 & 1) ? bfhi(r[i1 >> 1]) : bflo(r[i1 >> 1]);
            g2[c] = (i2 & 1) ? bfhi(r[i2 >> 1]) : bflo(r[i2 >> 1]);
            const float sm = g0[c] + g2[c];
            u1[c] = 0.5f * (sm + g1); u2[c] = 0.5f * (sm - g1);
        }
#pragma unroll
        for (int e = 0; e < 4; ++e) {
            o[dx][e] = pack2(g0[2 * e], g0[2 * e + 1]); o[3 + dx][e] = pack2(u1[2 * e], u1[2 * e + 1]);
            o[6 + dx][e] = pack2(u2[2 * e], u2[2 * e + 1]); o[9 + dx][e] = pack2(g2[2 * e], g2[2 * e + 1]);
        }
    }
}
#define CONV_OPERANDS \
        : \
        : [vA0] "v"(vA[0]), [vA1] "v"(vA[1]), [vA2] "v"(vA[2]), [vW] "v"(vW), [vE] "v"(vE), [vFo] "v"(vFo), [vRdy] "v"(vRdy), [vCnt] "v"(vCnt), \
          [wlo] "s"(wlo), [whi] "s"(whi), [alo] "s"(alo), [ahi] "s"(ahi), [mb] "s"(mb), [uoff] "s"(uoff), [ust2] "s"(ust2), [vst4] "s"(vst4), \
          [ucnt] "s"(ucnt), [vcnt] "s"(vcnt), [tag] "s"(tag), [fbase] "s"(fbase) \
        : "v31", "v32", "v33", "v34", "v35", "v36", "v37", "v38", "v39", "v40", "v41", "v42", "v43", "v44", "v45", "v46", "v47", "v48", "v49", "v50", "v51", "v52", "v53", "v54", "v55", "v56", "v57", "v58", "v59", "v60", "v61", "v62", "v63", "v64", "v65", "v66", "v67", "v68", "v69", "v70", "v71", "v72", "v73", "v74", "v75", "v76", "v77", "v78", "v79", "v80", "v81", "v82", "v83", "v84", "v85", "v86", "v87", "v88", "v89", "v90", "v91", "v92", "v93", "v94", "v95", "v96", "v97", "v98", "v99", "v100", "v101", "v102", "v103", "v104", "v105", "v106", "v107", "v108", "v109", "v110", "v111", "v112", "v113", "v114", "v115", "v116", "v117", "v118", "v119", "v120", "v121", "v122", "v123", "v124", "v125", "v126", "v127", "a0", "a1", "a2", "a3", "a4", "a5", "a6", "a7", "a8", "a9", "a10", "a11", "a12", "a13", "a14", "a15", "a16", "a17", "a18", "a19", "a20", "a21", "a22", "a23", "a24", "a25", "a26", "a27", "a28", "a29", "a30", "a31", "a32", "a33", "a34", "a35", "a36", "a37", "a38", "a39", "a40", "a41", "a42", "a43", "a44", "a45", "a46", "a47", "a48", "a49", "a50", "a51", "a52", "a53", "a54", "a55", "a56", "a57", "a58", "a59", "a60", "a61", "a62", "a63", "a64", "a65", "a66", "a67", "a68", "a69", "a70", "a71", "a72", "a73", "a74", "a75", "a76", "a77", "a78", "a79", "a80", "a81", "a82", "a83", "a84", "a85", "a86", "a87", "a88", "a89", "a90", "a91", "a92", "a93", "a94", "a95", "a96", "a97", "a98", "a99", "a100", "a101", "a102", "a103", "a104", "a105", "a106", "a107", "a108", "a109", "a110", "a111", "a112", "a113", "a114", "a115", "a116", "a117", "a118", "a119", "a120", "a121", "a122", "a123", "a124", "a125", "a126", "a127", "s60", "s61", "s62", "s63", "s64", "s65", "s66", "s67", "s68", "s69", "s70", "s71", "s72", "s73", "s74", "s75", "s76", "s77", "s78", "s79", "vcc", "scc", "memory"

template <bool CONV1>
DI void conv_main(char* lds, char* ws, int b, int j, int n, int wv, int lane, unsigned tag) {
    const unsigned ldsb = (unsigned)(size_t)(LAS char*)lds;
    const char* vbase = ws + (CONV1 ? OFF_V1 : OFF_V2) + ((((size_t)b * 32 + j * 2) * 4 + wv) * 65536);
    const char* ubase = ws + (CONV1 ? OFF_U1 : OFF_U2) + ((size_t)(n * 4 + wv) * 48) * 4096;
    unsigned vA[3];
#pragma unroll
    for (int dx = 0; dx < 3; ++dx) {
        const int px = (lane & 15) + dx;
        vA[dx] = ldsb + wv * WREG + px * 64 + (((lane >> 4) ^ (((px >> 2) & 1) << 1)) << 4);
    }
    const unsigned vW = lane * 16;
    const unsigned vE = CONV1 ? ldsb + wv * WREG + (lane & 15) * E1_PITCH + (lane >> 4) * 16
                              : ldsb + wv * WREG + (lane & 15) * 64 + (((lane >> 4) ^ ((lane >> 1) & 3)) << 4);
    const int ptile = b * 16 + j;
    const unsigned vFo = (unsigned)((ptile * 8 + n) * 128), vRdy = ldsb + LDS_SCR + ((CONV1 ? 244 : 248) + wv) * 4;
    const unsigned vCnt = ldsb + LDS_SCR + (CONV1 ? 240 : 241) * 4;
    const unsigned long long fbase = (unsigned long long)(size_t)(ws + (CONV1 ? OFF_FV1 : OFF_FV2));
    const unsigned mb = __builtin_amdgcn_readfirstlane(ldsb + wv * WREG);
    const unsigned wlo = __builtin_amdgcn_readfirstlane((unsigned)(size_t)ubase), whi = __builtin_amdgcn_readfirstlane((unsigned)((size_t)ubase >> 32));
    const unsigned alo = __builtin_amdgcn_readfirstlane((unsigned)(size_t)vbase), ahi = __builtin_amdgcn_readfirstlane((unsigned)((size_t)vbase >> 32));
    const unsigned uoff = (unsigned)(n * 6 * 4096), ust2 = n ? 0u : 24576u;
    const unsigned vst4 = (unsigned)((2 < 2 * n ? 2 : 4) * 4096);
    const unsigned ucnt = n ? (unsigned)(6 * n - 1) : 1000u, vcnt = n >= 2 ? (unsigned)(2 * n - 3) : 1000u;
    if (CONV1) {
        asm volatile(
    "s_mov_b32 s60, %[wlo]\n"
    "s_and_b32 s61, %[whi], 0xffff\n"
    "s_mov_b32 s62, 196608\n"
    "s_mov_b32 s63, 0x20000\n"
    "s_mov_b32 s68, %[uoff]\n"
    "buffer_load_dwordx4 v[64:67], %[vW], s[60:63], s68 offen offset:0 sc1\n"
    "buffer_load_dwordx4 v[68:71], %[vW], s[60:63], s68 offen offset:1024 sc1\n"
    "buffer_load_dwordx4 v[72:75], %[vW], s[60:63], s68 offen offset:2048 sc1\n"
    "buffer_load_dwordx4 v[76:79], %[vW], s[60:63], s68 offen offset:3072 sc1\n"
    "s_add_u32 s68, s68, 4096\n"
    "buffer_load_dwordx4 v[80:83], %[vW], s[60:63], s68 offen offset:0 sc1\n"
    "buffer_load_dwordx4 v[84:87], %[vW], s[60:63], s68 offen offset:1024 sc1\n"
    "buffer_load_dwordx4 v[88:91], %[vW], s[60:63], s68 offen offset:2048 sc1\n"
    "buffer_load_dwordx4 v[92:95], %[vW], s[60:63], s68 offen offset:3072 sc1\n"
    "s_add_u32 s68, s68, 4096\n"
    "buffer_load_dwordx4 v[96:99], %[vW], s[60:63], s68 offen offset:0 sc1\n"
    "buffer_load_dwordx4 v[100:103], %[vW], s[60:63], s68 offen offset:1024 sc1\n"
    "buffer_load_dwordx4 v[104:107], %[vW], s[60:63], s68 offen offset:2048 sc1\n"
    "buffer_load_dwordx4 v[108:111], %[vW], s[60:63], s68 offen offset:3072 sc1\n"
    "s_add_u32 s68, s68, 4096\n"
    "s_mov_b32 s64, %[alo]\n"
    "s_and_b32 s65, %[ahi], 0xffff\n"
    "s_mov_b32 s66, 327680\n"
    "s_mov_b32 s67, 0x20000\n"
    "s_mov_b32 s69, %[vst4]\n"
    "s_mov_b32 s71, 0\n"
    "s_mov_b32 s74, %[ucnt]\n"
    "s_mov_b32 s75, %[vcnt]\n"
    "s_mov_b32 s76, 4096\n"
    "s_mov_b32 s77, 28672\n"
    "s_mov_b32 s78, 12288\n"
    "s_waitcnt lgkmcnt(0)\n"
    "ds_read_b128 v[32:35], %[vA0] offset:0\n"
    "ds_read_b128 v[36:39], %[vA0] offset:1024\n"
    "ds_read_b128 v[40:43], %[vA0] offset:2048\n"
    "ds_read_b128 v[44:47], %[vA0] offset:3072\n"
    "ds_read_b128 v[48:51], %[vA0] offset:4224\n"
    "ds_read_b128 v[52:55], %[vA0] offset:5248\n"
    "ds_read_b128 v[56:59], %[vA0] offset:6272\n"
    "ds_read_b128 v[60:63], %[vA0] offset:7296\n"
    "s_waitcnt vmcnt(0)\n"
    "s_mov_b64 exec, 1\n"
    "v_mov_b32 v31, 1\n"
    "ds_add_rtn_u32 v31, %[vCnt], v31\n"
    "s_waitcnt lgkmcnt(0)\n"
    "v_cmp_eq_u32 vcc, 7, v31\n"
    "v_mov_b32 v31, %[tag]\n"
    "s_cbranch_vccz NOFLAG%=\n"
    "global_store_dword %[vFo], v31, %[fbase] sc1\n"
    "NOFLAG%=:\n"
    "s_mov_b64 exec, -1\n"
    "s_nop 4\n"
    "s_waitcnt vmcnt(8) lgkmcnt(6)\n"
    "v_mfma_f32_16x16x32_bf16 a[0:3], v[64:67], v[32:35], 0\n"
    "v_mfma_f32_16x16x32_bf16 a[4:7], v[68:71], v[32:35], 0\n"
    "v_mfma_f32_16x16x32_bf16 a[8:11], v[72:75], v[32:35], 0\n"
    "v_mfma_f32_16x16x32_bf16 a[12:15], v[76:79], v[32:35], 0\n"
    "ds_read_b128 v[32:35], %[vA1] offset:0\n"
    "v_mfma_f32_16x16x32_bf16 a[16:19], v[64:67], v[36:39], 0\n"
    "v_mfma_f32_16x16x32_bf16 a[20:23], v[68:71], v[36:39], 0\n"
    "buffer_load_dwordx4 v[112:115], %[vW], s[60:63], s68 offen offset:0 sc1\n"
    "v_mfma_f32_16x16x32_bf16 a[24:27], v[72:75], v[36:39], 0\n"
    "v_mfma_f32_16x16x32_bf16 a[28:31], v[76:79], v[36:39], 0\n"
    "ds_read_b128 v[36:39], %[vA1] offset:1024\n"
    "s_waitcnt lgkmcnt(6)\n"
    "v_mfma_f32_16x16x32_bf16 a[32:35], v[64:67], v[40:43], 0\n"
    "v_mfma_f32_16x16x32_bf16 a[36:39], v[68:71], v[40:43], 0\n"
    "v_mfma_f32_16x16x32_bf16 a[40:43], v[72:75], v[40:43], 0\n"
    "v_mfma_f32_16x16x32_bf16 a[44:47], v[76:79], v[40:43], 0\n"
    "ds_read_b128 v[40:43], %[vA1] offset:2048\n"
    "v_mfma_f32_16x16x32_bf16 a[48:51], v[64:67], v[44:47], 0\n"
    "v_mfma_f32_16x16x32_bf16 a[52:55], v[68:71], v[44:47], 0\n"
    "buffer_load_dwordx4 v[116:119], %[vW], s[60:63], s68 offen offset:1024 sc1\n"
    "v_mfma_f32_16x16x32_bf16 a[56:59], v[72:75], v[44:47], 0\n"
    "v_mfma_f32_16x16x32_bf16 a[60:63], v[76:79], v[44:47], 0\n"
    "ds_read_b128 v[44:47], %[vA1] offset:3072\n"
    "s_waitcnt lgkmcnt(6)\n"
    "v_mfma_f32_16x16x32_bf16 a[64:67], v[64:67], v[48:51], 0\n"
    "v_mfma_f32_16x16x32_bf16 a[68:71], v[68:71], v[48:51], 0\n"
    "v_mfma_f32_16x16x32_bf16 a[72:75], v[72:75], v[48:51], 0\n"
    "v_mfma_f32_16x16x32_bf16 a[76:79], v[76:79], v[48:51], 0\n"
    "ds_read_b128 v[48:51], %[vA1] offset:4224\n"
    "v_mfma_f32_16x16x32_bf16 a[80:83], v[64:67], v[52:55], 0\n"
    "v_mfma_f32_16x16x32_bf16 a[84:87], v[68:71], v[52:55], 0\n"
    "buffer_load_dwordx4 v[120:123], %[vW], s[60:63], s68 offen offset:2048 sc1\n"
    "v_mfma_f32_16x16x32_bf16 a[88:91], v[72:75], v[52:55], 0\n"
    "v_mfma_f32_16x16x32_bf16 a[92:95], v[76:79], v[52:55], 0\n"
    "ds_read_b128 v[52:55], %[vA1] offset:5248\n"
    "s_waitcnt lgkmcnt(6)\n"
    "v_mfma_f32_16x16x32_bf16 a[96:99], v[64:67], v[56:59], 0\n"
    "v_mfma_f32_16x16x32_bf16 a[100:103], v[68:71], v[56:59], 0\n"
    "v_mfma_f32_16x16x32_bf16 a[104:107], v[72:75], v[56:59], 0\n"
    "buffer_load_dwordx4 v[124:127], %[vW], s[60:63], s68 offen offset:3072 sc1\n"
    "v_mfma_f32_16x16x32_bf16 a[108:111], v[76:79], v[56:59], 0\n"
    "ds_read_b128 v[56:59], %[vA1] offset:6272\n"
    "v_mfma_f32_16x16x32_bf16 a[112:115], v[64:67], v[60:63], 0\n"
    "s_add_u32 s68, s68, 4096\n"
    "v_mfma_f32_16x16x32_bf16 a[116:119], v[68:71], v[60:63], 0\n"
    "v_mfma_f32_16x16x32_bf16 a[120:123], v[72:75], v[60:63], 0\n"
    "v_mfma_f32_16x16x32_bf16 a[124:127], v[76:79], v[60:63], 0\n"
    "ds_read_b128 v[60:63], %[vA1] offset:7296\n"
    "s_waitcnt vmcnt(8) lgkmcnt(6)\n"
    "v_mfma_f32_16x16x32_bf16 a[0:3], v[80:83], v[32:35], a[0:3]\n"
    "v_mfma_f32_16x16x32_bf16 a[4:7], v[84:87], v[32:35], a[4:7]\n"
    "v_mfma_f32_16x16x32_bf16 a[8:11], v[88:91], v[32:35], a[8:11]\n"
    "v_mfma_f32_16x16x32_bf16 a[12:15], v[92:95], v[32:35], a[12:15]\n"
    "ds_read_b128 v[32:35], %[vA2] offset:0\n"
    "v_mfma_f32_16x16x32_bf16 a[16:19], v[80:83], v[36:39], a[16:19]\n"
    "v_mfma_f32_16x16x32_bf16 a[20:23], v[84:87], v[36:39], a[20:23]\n"
    "buffer_load_dwordx4 v[64:67], %[vW], s[60:63], s68 offen offset:0 sc1\n"
    "v_mfma_f32_16x16x32_bf16 a[24:27], v[88:91], v[36:39], a[24:27]\n"
    "v_mfma_f32_16x16x32_bf16 a[28:31], v[92:95], v[36:39], a[28:31]\n"
    "ds_read_b128 v[36:39], %[vA2] offset:1024\n"
    "s_waitcnt lgkmcnt(6)\n"
    "v_mfma_f32_16x16x32_bf16 a[32:35], v[80:83], v[40:43], a[32:35]\n"
    "v_mfma_f32_16x16x32_bf16 a[36:39], v[84:87], v[40:43], a[36:39]\n"
    "v_mfma_f32_16x16x32_bf16 a[40:43], v[88:91], v[40:43], a[40:43]\n"
    "v_mfma_f32_16x16x32_bf16 a[44:47], v[92:95], v[40:43], a[44:47]\n"
    "ds_read_b128 v[40:43], %[vA2] offset:2048\n"
    "v_mfma_f32_16x16x32_bf16 a[48:51], v[80:83], v[44:47], a[48:51]\n"
    "v_mfma_f32_16x16x32_bf16 a[52:55], v[84:87], v[44:47], a[52:55]\n"
    "buffer_load_dwordx4 v[68:71], %[vW], s[60:63], s68 offen offset:1024 sc1\n"
    "v_mfma_f32_16x16x32_bf16 a[56:59], v[88:91], v[44:47], a[56:59]\n"
    "v_mfma_f32_16x16x32_bf16 a[60:63], v[92:95], v[44:47], a[60:63]\n"
    "ds_read_b128 v[44:47], %[vA2] offset:3072\n"
    "s_waitcnt lgkmcnt(6)\n"
    "v_mfma_f32_16x16x32_bf16 a[64:67], v[80:83], v[48:51], a[64:67]\n"
    "v_mfma_f32_16x16x32_bf16 a[68:71], v[84:87], v[48:51], a[68:71]\n"
    "v_mfma_f32_16x16x32_bf16 a[72:75], v[88:91], v[48:51], a[72:75]\n"
    "v_mfma_f32_16x16x32_bf16 a[76:79], v[92:95], v[48:51], a[76:79]\n"
    "ds_read_b128 v[48:51], %[vA2] offset:4224\n"
    "v_mfma_f32_16x16x32_bf16 a[80:83], v[80:83], v[52:55], a[80:83]\n"
    "v_mfma_f32_16x16x32_bf16 a[84:87], v[84:87], v[52:55], a[84:87]\n"
    "buffer_load_dwordx4 v[72:75], %[vW], s[60:63], s68 offen offset:2048 sc1\n"
    "v_mfma_f32_16x16x32_bf16 a[88:91], v[88:91], v[52:55], a[88:91]\n"
    "v_mfma_f32_16x16x32_bf16 a[92:95], v[92:95], v[52:55], a[92:95]\n"
    "ds_read_b128 v[52:55], %[vA2] offset:5248\n"
    "s_waitcnt lgkmcnt(6)\n"
    "v_mfma_f32_16x16x32_bf16 a[96:99], v[80:83], v[56:59], a[96:99]\n"
    "v_mfma_f32_16x16x32_bf16 a[100:103], v[84:87], v[56:59], a[100:103]\n"
    "v_mfma_f32_16x16x32_bf16 a[104:107], v[88:91], v[56:59], a[104:107]\n"
    "buffer_load_dwordx4 v[76:79], %[vW], s[60:63], s68 offen offset:3072 sc1\n"
    "v_mfma_f32_16x16x32_bf16 a[108:111], v[92:95], v[56:59], a[108:111]\n"
    "ds_read_b128 v[56:59], %[vA2] offset:6272\n"
    "v_mfma_f32_16x16x32_bf16 a[112:115], v[80:83], v[60:63], a[112:115]\n"
    "s_add_u32 s68, s68, 4096\n"
    "v_mfma_f32_16x16x32_bf16 a[116:119], v[84:87], v[60:63], a[116:119]\n"
    "v_mfma_f32_16x16x32_bf16 a[120:123], v[88:91], v[60:63], a[120:123]\n"
    "v_mfma_f32_16x16x32_bf16 a[124:127], v[92:95], v[60:63], a[124:127]\n"
    "ds_read_b128 v[60:63], %[vA2] offset:7296\n"
    "s_waitcnt vmcnt(8) lgkmcnt(6)\n"
    "v_mfma_f32_16x16x32_bf16 a[0:3], v[96:99], v[32:35], a[0:3]\n"
    "v_mfma_f32_16x16x32_bf16 a[4:7], v[100:103], v[32:35], a[4:7]\n"
    "v_mfma_f32_16x16x32_bf16 a[8:11], v[104:107], v[32:35], a[8:11]\n"
    "v_mfma_f32_16x16x32_bf16 a[12:15], v[108:111], v[32:35], a[12:15]\n"
    "ds_read_b128 v[32:35], %[vA0] offset:8448\n"
    "v_mfma_f32_16x16x32_bf16 a[16:19], v[96:99], v[36:39], a[16:19]\n"
    "v_mfma_f32_16x16x32_bf16 a[20:23], v[100:103], v[36:39], a[20:23]\n"
    "buffer_load_dwordx4 v[80:83], %[vW], s[60:63], s68 offen offset:0 sc1\n"
    "v_mfma_f32_16x16x32_bf16 a[24:27], v[104:107], v[36:39], a[24:27]\n"
    "v_mfma_f32_16x16x32_bf16 a[28:31], v[108:111], v[36:39], a[28:31]\n"
    "ds_read_b128 v[36:39], %[vA0] offset:9472\n"
    "s_waitcnt lgkmcnt(6)\n"
    "v_mfma_f32_16x16x32_bf16 a[32:35], v[96:99], v[40:43], a[32:35]\n"
    "v_mfma_f32_16x16x32_bf16 a[36:39], v[100:103], v[40:43], a[36:39]\n"
    "v_mfma_f32_16x16x32_bf16 a[40:43], v[104:107], v[40:43], a[40:43]\n"
    "v_mfma_f32_16x16x32_bf16 a[44:47], v[108:111], v[40:43], a[44:47]\n"
    "ds_read_b128 v[40:43], %[vA0] offset:10496\n"
    "v_mfma_f32_16x16x32_bf16 a[48:51], v[96:99], v[44:47], a[48:51]\n"
    "v_mfma_f32_16x16x32_bf16 a[52:55], v[100:103], v[44:47], a[52:55]\n"
    "buffer_load_dwordx4 v[84:87], %[vW], s[60:63], s68 offen offset:1024 sc1\n"
    "v_mfma_f32_16x16x32_bf16 a[56:59], v[104:107], v[44:47], a[56:59]\n"
    "v_mfma_f32_16x16x32_bf16 a[60:63], v[108:111], v[44:47], a[60:63]\n"
    "ds_read_b128 v[44:47], %[vA0] offset:11520\n"
    "s_waitcnt lgkmcnt(6)\n"
    "v_mfma_f32_16x16x32_bf16 a[64:67], v[96:99], v[48:51], a[64:67]\n"
    "v_mfma_f32_16x16x32_bf16 a[68:71], v[100:103], v[48:51], a[68:71]\n"
    "v_mfma_f32_16x16x32_bf16 a[72:75], v[104:107], v[48:51], a[72:75]\n"
    "v_mfma_f32_16x16x32_bf16 a[76:79], v[108:111], v[48:51], a[76:79]\n"
    "ds_read_b128 v[48:51], %[vA0] offset:12672\n"
    "v_mfma_f32_16x16x32_bf16 a[80:83], v[96:99], v[52:55], a[80:83]\n"
    "v_mfma_f32_16x16x32_bf16 a[84:87], v[100:103], v[52:55], a[84:87]\n"
    "buffer_load_dwordx4 v[88:91], %[vW], s[60:63], s68 offen offset:2048 sc1\n"
    "v_mfma_f32_16x16x32_bf16 a[88:91], v[104:107], v[52:55], a[88:91]\n"
    "v_mfma_f32_16x16x32_bf16 a[92:95], v[108:111], v[52:55], a[92:95]\n"
    "ds_read_b128 v[52:55], %[vA0] offset:13696\n"
    "s_waitcnt lgkmcnt(6)\n"
    "v_mfma_f32_16x16x32_bf16 a[96:99], v[96:99], v[56:59], a[96:99]\n"
    "v_mfma_f32_16x16x32_bf16 a[100:103], v[100:103], v[56:59], a[100:103]\n"
    "v_mfma_f32_16x16x32_bf16 a[104:107], v[104:107], v[56:59], a[104:107]\n"
    "buffer_load_dwordx4 v[92:95], %[vW], s[60:63], s68 offen offset:3072 sc1\n"
    "v_mfma_f32_16x16x32_bf16 a[108:111], v[108:111], v[56:59], a[108:111]\n"
    "ds_read_b128 v[56:59], %[vA0] offset:14720\n"
    "v_mfma_f32_16x16x32_bf16 a[112:115], v[96:99], v[60:63], a[112:115]\n"
    "s_mov_b32 s68, %[ust2]\n"
    "v_mfma_f32_16x16x32_bf16 a[116:119], v[100:103], v[60:63], a[116:119]\n"
    "v_mfma_f32_16x16x32_bf16 a[120:123], v[104:107], v[60:63], a[120:123]\n"
    "v_mfma_f32_16x16x32_bf16 a[124:127], v[108:111], v[60:63], a[124:127]\n"
    "ds_read_b128 v[60:63], %[vA0] offset:15744\n"
    "s_waitcnt vmcnt(8) lgkmcnt(6)\n"
    "v_mfma_f32_16x16x32_bf16 a[0:3], v[112:115], v[32:35], a[0:3]\n"
    "v_mfma_f32_16x16x32_bf16 a[4:7], v[116:119], v[32:35], a[4:7]\n"
    "v_mfma_f32_16x16x32_bf16 a[8:11], v[120:123], v[32:35], a[8:11]\n"
    "v_mfma_f32_16x16x32_bf16 a[12:15], v[124:127], v[32:35], a[12:15]\n"
    "ds_read_b128 v[32:35], %[vA1] offset:8448\n"
    "v_mfma_f32_16x16x32_bf16 a[16:19], v[112:115], v[36:39], a[16:19]\n"
    "v_mfma_f32_16x16x32_bf16 a[20:23], v[116:119], v[36:39], a[20:23]\n"
    "buffer_load_dwordx4 v[96:99], %[vW], s[60:63], s68 offen offset:0 sc1\n"
    "v_mfma_f32_16x16x32_bf16 a[24:27], v[120:123], v[36:39], a[24:27]\n"
    "v_mfma_f32_16x16x32_bf16 a[28:31], v[124:127], v[36:39], a[28:31]\n"
    "ds_read_b128 v[36:39], %[vA1] offset:9472\n"
    "s_waitcnt lgkmcnt(6)\n"
    "v_mfma_f32_16x16x32_bf16 a[32:35], v[112:115], v[40:43], a[32:35]\n"
    "v_mfma_f32_16x16x32_bf16 a[36:39], v[116:119], v[40:43], a[36:39]\n"
    "v_mfma_f32_16x16x32_bf16 a[40:43], v[120:123], v[40:43], a[40:43]\n"
    "v_mfma_f32_16x16x32_bf16 a[44:47], v[124:127], v[40:43], a[44:47]\n"
    "ds_read_b128 v[40:43], %[vA1] offset:10496\n"
    "v_mfma_f32_16x16x32_bf16 a[48:51], v[112:115], v[44:47], a[48:51]\n"
    "v_mfma_f32_16x16x32_bf16 a[52:55], v[116:119], v[44:47], a[52:55]\n"
    "buffer_load_dwordx4 v[100:103], %[vW], s[60:63], s68 offen offset:1024 sc1\n"
    "v_mfma_f32_16x16x32_bf16 a[56:59], v[120:123], v[44:47], a[56:59]\n"
    "v_mfma_f32_16x16x32_bf16 a[60:63], v[124:127], v[44:47], a[60:63]\n"
    "ds_read_b128 v[44:47], %[vA1] offset:11520\n"
    "s_waitcnt lgkmcnt(6)\n"
    "v_mfma_f32_16x16x32_bf16 a[64:67], v[112:115], v[48:51], a[64:67]\n"
    "v_mfma_f32_16x16x32_bf16 a[68:71], v[116:119], v[48:51], a[68:71]\n"
    "v_mfma_f32_16x16x32_bf16 a[72:75], v[120:123], v[48:51], a[72:75]\n"
    "v_mfma_f32_16x16x32_bf16 a[76:79], v[124:127], v[48:51], a[76:79]\n"
    "ds_read_b128 v[48:51], %[vA1] offset:12672\n"
    "v_mfma_f32_16x16x32_bf16 a[80:83], v[112:115], v[52:55], a[80:83]\n"
    "v_mfma_f32_16x16x32_bf16 a[84:87], v[116:119], v[52:55], a[84:87]\n"
    "buffer_load_dwordx4 v[104:107], %[vW], s[60:63], s68 offen offset:2048 sc1\n"
    "v_mfma_f32_16x16x32_bf16 a[88:91], v[120:123], v[52:55], a[88:91]\n"
    "v_mfma_f32_16x16x32_bf16 a[92:95], v[124:127], v[52:55], a[92:95]\n"
    "ds_read_b128 v[52:55], %[vA1] offset:13696\n"
    "s_waitcnt lgkmcnt(6)\n"
    "v_mfma_f32_16x16x32_bf16 a[96:99], v[112:115], v[56:59], a[96:99]\n"
    "v_mfma_f32_16x16x32_bf16 a[100:103], v[116:119], v[56:59], a[100:103]\n"
    "v_mfma_f32_16x16x32_bf16 a[104:107], v[120:123], v[56:59], a[104:107]\n"
    "buffer_load_dwordx4 v[108:111], %[vW], s[60:63], s68 offen offset:3072 sc1\n"
    "v_mfma_f32_16x16x32_bf16 a[108:111], v[124:127], v[56:59], a[108:111]\n"
    "ds_read_b128 v[56:59], %[vA1] offset:14720\n"
    "v_mfma_f32_16x16x32_bf16 a[112:115], v[112:115], v[60:63], a[112:115]\n"
    "s_sub_u32 s74, s74, 1\n"
    "s_cselect_b32 s73, s77, s76\n"
    "v_mfma_f32_16x16x32_bf16 a[116:119], v[116:119], v[60:63], a[116:119]\n"
    "s_add_u32 s68, s68, s73\n"
    "v_mfma_f32_16x16x32_bf16 a[120:123], v[120:123], v[60:63], a[120:123]\n"
    "v_mfma_f32_16x16x32_bf16 a[124:127], v[124:127], v[60:63], a[124:127]\n"
    "ds_read_b128 v[60:63], %[vA1] offset:15744\n"
    "s_waitcnt vmcnt(8) lgkmcnt(6)\n"
    "v_mfma_f32_16x16x32_bf16 a[0:3], v[64:67], v[32:35], a[0:3]\n"
    "v_mfma_f32_16x16x32_bf16 a[4:7], v[68:71], v[32:35], a[4:7]\n"
    "v_mfma_f32_16x16x32_bf16 a[8:11], v[72:75], v[32:35], a[8:11]\n"
    "v_mfma_f32_16x16x32_bf16 a[12:15], v[76:79], v[32:35], a[12:15]\n"
    "ds_read_b128 v[32:35], %[vA2] offset:8448\n"
    "v_mfma_f32_16x16x32_bf16 a[16:19], v[64:67], v[36:39], a[16:19]\n"
    "v_mfma_f32_16x16x32_bf16 a[20:23], v[68:71], v[36:39], a[20:23]\n"
    "buffer_load_dwordx4 v[112:115], %[vW], s[60:63], s68 offen offset:0 sc1\n"
    "v_mfma_f32_16x16x32_bf16 a[24:27], v[72:75], v[36:39], a[24:27]\n"
    "v_mfma_f32_16x16x32_bf16 a[28:31], v[76:79], v[36:39], a[28:31]\n"
    "ds_read_b128 v[36:39], %[vA2] offset:9472\n"
    "s_waitcnt lgkmcnt(6)\n"
    "v_mfma_f32_16x16x32_bf16 a[32:35], v[64:67], v[40:43], a[32:35]\n"
    "v_mfma_f32_16x16x32_bf16 a[36:39], v[68:71], v[40:43], a[36:39]\n"
    "v_mfma_f32_16x16x32_bf16 a[40:43], v[72:75], v[40:43], a[40:43]\n"
    "v_mfma_f32_16x16x32_bf16 a[44:47], v[76:79], v[40:43], a[44:47]\n"
    "ds_read_b128 v[40:43], %[vA2] offset:10496\n"
    "v_mfma_f32_16x16x32_bf16 a[48:51], v[64:67], v[44:47], a[48:51]\n"
    "v_mfma_f32_16x16x32_bf16 a[52:55], v[68:71], v[44:47], a[52:55]\n"
    "buffer_load_dwordx4 v[116:119], %[vW], s[60:63], s68 offen offset:1024 sc1\n"
    "v_mfma_f32_16x16x32_bf16 a[56:59], v[72:75], v[44:47], a[56:59]\n"
    "v_mfma_f32_16x16x32_bf16 a[60:63], v[76:79], v[44:47], a[60:63]\n"
    "ds_read_b128 v[44:47], %[vA2] offset:11520\n"
    "s_waitcnt lgkmcnt(6)\n"
    "v_mfma_f32_16x16x32_bf16 a[64:67], v[64:67], v[48:51], a[64:67]\n"
    "v_mfma_f32_16x16x32_bf16 a[68:71], v[68:71], v[48:51], a[68:71]\n"
    "v_mfma_f32_16x16x32_bf16 a[72:75], v[72:75], v[48:51], a[72:75]\n"
    "v_mfma_f32_16x16x32_bf16 a[76:79], v[76:79], v[48:51], a[76:79]\n"
    "ds_read_b128 v[48:51], %[vA2] offset:12672\n"
    "v_mfma_f32_16x16x32_bf16 a[80:83], v[64:67], v[52:55], a[80:83]\n"
    "v_mfma_f32_16x16x32_bf16 a[84:87], v[68:71], v[52:55], a[84:87]\n"
    "buffer_load_dwordx4 v[120:123], %[vW], s[60:63], s68 offen offset:2048 sc1\n"
    "v_mfma_f32_16x16x32_bf16 a[88:91], v[72:75], v[52:55], a[88:91]\n"
    "v_mfma_f32_16x16x32_bf16 a[92:95], v[76:79], v[52:55], a[92:95]\n"
    "ds_read_b128 v[52:55], %[vA2] offset:13696\n"
    "s_waitcnt lgkmcnt(6)\n"
    "v_mfma_f32_16x16x32_bf16 a[96:99], v[64:67], v[56:59], a[96:99]\n"
    "v_mfma_f32_16x16x32_bf16 a[100:103], v[68:71], v[56:59], a[100:103]\n"
    "v_mfma_f32_16x16x32_bf16 a[104:107], v[72:75], v[56:59], a[104:107]\n"
    "buffer_load_dwordx4 v[124:127], %[vW], s[60:63], s68 offen offset:3072 sc1\n"
    "v_mfma_f32_16x16x32_bf16 a[108:111], v[76:79], v[56:59], a[108:111]\n"
    "ds_read_b128 v[56:59], %[vA2] offset:14720\n"
    "v_mfma_f32_16x16x32_bf16 a[112:115], v[64:67], v[60:63], a[112:115]\n"
    "s_sub_u32 s74, s74, 1\n"
    "s_cselect_b32 s73, s77, s76\n"
    "v_mfma_f32_16x16x32_bf16 a[116:119], v[68:71], v[60:63], a[116:119]\n"
    "s_add_u32 s68, s68, s73\n"
    "v_mfma_f32_16x16x32_bf16 a[120:123], v[72:75], v[60:63], a[120:123]\n"
    "v_mfma_f32_16x16x32_bf16 a[124:127], v[76:79], v[60:63], a[124:127]\n"
    "ds_read_b128 v[60:63], %[vA2] offset:15744\n"
    "s_waitcnt vmcnt(8) lgkmcnt(6)\n"
    "v_mfma_f32_16x16x32_bf16 a[0:3], v[80:83], v[32:35], a[0:3]\n"
    "v_mfma_f32_16x16x32_bf16 a[4:7], v[84:87], v[32:35], a[4:7]\n"
    "v_mfma_f32_16x16x32_bf16 a[8:11], v[88:91], v[32:35], a[8:11]\n"
    "v_mfma_f32_16x16x32_bf16 a[12:15], v[92:95], v[32:35], a[12:15]\n"
    "v_mfma_f32_16x16x32_bf16 a[16:19], v[80:83], v[36:39], a[16:19]\n"
    "v_mfma_f32_16x16x32_bf16 a[20:23], v[84:87], v[36:39], a[20:23]\n"
    "buffer_load_dwordx4 v[64:67], %[vW], s[60:63], s68 offen offset:0 sc1\n"
    "v_mfma_f32_16x16x32_bf16 a[24:27], v[88:91], v[36:39], a[24:27]\n"
    "v_mfma_f32_16x16x32_bf16 a[28:31], v[92:95], v[36:39], a[28:31]\n"
    "s_waitcnt lgkmcnt(4)\n"
    "v_mfma_f32_16x16x32_bf16 a[32:35], v[80:83], v[40:43], a[32:35]\n"
    "v_mfma_f32_16x16x32_bf16 a[36:39], v[84:87], v[40:43], a[36:39]\n"
    "v_mfma_f32_16x16x32_bf16 a[40:43], v[88:91], v[40:43], a[40:43]\n"
    "v_mfma_f32_16x16x32_bf16 a[44:47], v[92:95], v[40:43], a[44:47]\n"
    "v_mfma_f32_16x16x32_bf16 a[48:51], v[80:83], v[44:47], a[48:51]\n"
    "v_mfma_f32_16x16x32_bf16 a[52:55], v[84:87], v[44:47], a[52:55]\n"
    "buffer_load_dwordx4 v[68:71], %[vW], s[60:63], s68 offen offset:1024 sc1\n"
    "v_mfma_f32_16x16x32_bf16 a[56:59], v[88:91], v[44:47], a[56:59]\n"
    "v_mfma_f32_16x16x32_bf16 a[60:63], v[92:95], v[44:47], a[60:63]\n"
    "s_waitcnt lgkmcnt(2)\n"
    "v_mfma_f32_16x16x32_bf16 a[64:67], v[80:83], v[48:51], a[64:67]\n"
    "v_mfma_f32_16x16x32_bf16 a[68:71], v[84:87], v[48:51], a[68:71]\n"
    "v_mfma_f32_16x16x32_bf16 a[72:75], v[88:91], v[48:51], a[72:75]\n"
    "v_mfma_f32_16x16x32_bf16 a[76:79], v[92:95], v[48:51], a[76:79]\n"
    "v_mfma_f32_16x16x32_bf16 a[80:83], v[80:83], v[52:55], a[80:83]\n"
    "v_mfma_f32_16x16x32_bf16 a[84:87], v[84:87], v[52:55], a[84:87]\n"
    "buffer_load_dwordx4 v[72:75], %[vW], s[60:63], s68 offen offset:2048 sc1\n"
    "v_mfma_f32_16x16x32_bf16 a[88:91], v[88:91], v[52:55], a[88:91]\n"
    "v_mfma_f32_16x16x32_bf16 a[92:95], v[92:95], v[52:55], a[92:95]\n"
    "s_waitcnt lgkmcnt(0)\n"
    "v_mfma_f32_16x16x32_bf16 a[96:99], v[80:83], v[56:59], a[96:99]\n"
    "v_mfma_f32_16x16x32_bf16 a[100:103], v[84:87], v[56:59], a[100:103]\n"
    "v_mfma_f32_16x16x32_bf16 a[104:107], v[88:91], v[56:59], a[104:107]\n"
    "buffer_load_dwordx4 v[76:79], %[vW], s[60:63], s68 offen offset:3072 sc1\n"
    "v_mfma_f32_16x16x32_bf16 a[108:111], v[92:95], v[56:59], a[108:111]\n"
    "s_sub_u32 s74, s74, 1\n"
    "s_cselect_b32 s73, s77, s76\n"
    "v_mfma_f32_16x16x32_bf16 a[112:115], v[80:83], v[60:63], a[112:115]\n"
    "s_add_u32 s68, s68, s73\n"
    "v_mfma_f32_16x16x32_bf16 a[116:119], v[84:87], v[60:63], a[116:119]\n"
    "v_mfma_f32_16x16x32_bf16 a[120:123], v[88:91], v[60:63], a[120:123]\n"
    "v_mfma_f32_16x16x32_bf16 a[124:127], v[92:95], v[60:63], a[124:127]\n"
    "s_mov_b32 s72, 0\n"
    "POLL%=:\n"
    "ds_read_b32 v31, %[vRdy]\n"
    "s_waitcnt lgkmcnt(0)\n"
    "v_cmp_ne_u32 vcc, 0, v31\n"
    "s_cbranch_vccnz PDONE%=\n"
    "s_add_u32 s72, s72, 1\n"
    "s_cmp_gt_u32 s72, 4194304\n"
    "s_cbranch_scc1 PDONE%=\n"
    "s_branch POLL%=\n"
    "PDONE%=:\n"
    "ds_read_b128 v[32:35], %[vA0] offset:16896\n"
    "ds_read_b128 v[36:39], %[vA0] offset:17920\n"
    "ds_read_b128 v[40:43], %[vA0] offset:18944\n"
    "ds_read_b128 v[44:47], %[vA0] offset:19968\n"
    "ds_read_b128 v[48:51], %[vA0] offset:21120\n"
    "ds_read_b128 v[52:55], %[vA0] offset:22144\n"
    "ds_read_b128 v[56:59], %[vA0] offset:23168\n"
    "ds_read_b128 v[60:63], %[vA0] offset:24192\n"
    "s_add_u32 m0, %[mb], 64\n"
    "s_add_u32 s70, s69, 0\n"
    "buffer_load_dwordx4 %[vW], s[64:67], s70 offen sc1 lds\n"
    "s_add_u32 m0, %[mb], 1088\n"
    "s_add_u32 s70, s69, 1024\n"
    "buffer_load_dwordx4 %[vW], s[64:67], s70 offen sc1 lds\n"
    "s_add_u32 m0, %[mb], 2112\n"
    "s_add_u32 s70, s69, 2048\n"
    "buffer_load_dwordx4 %[vW], s[64:67], s70 offen sc1 lds\n"
    "s_add_u32 m0, %[mb], 3136\n"
    "s_add_u32 s70, s69, 3072\n"
    "buffer_load_dwordx4 %[vW], s[64:67], s70 offen sc1 lds\n"
    "s_add_u32 m0, %[mb], 4288\n"
    "s_add_u32 s70, s69, 262144\n"
    "buffer_load_dwordx4 %[vW], s[64:67], s70 offen sc1 lds\n"
    "s_add_u32 m0, %[mb], 5312\n"
    "s_add_u32 s70, s69, 263168\n"
    "buffer_load_dwordx4 %[vW], s[64:67], s70 offen sc1 lds\n"
    "s_add_u32 m0, %[mb], 6336\n"
    "s_add_u32 s70, s69, 264192\n"
    "buffer_load_dwordx4 %[vW], s[64:67], s70 offen sc1 lds\n"
    "s_add_u32 m0, %[mb], 7360\n"
    "s_add_u32 s70, s69, 265216\n"
    "buffer_load_dwordx4 %[vW], s[64:67], s70 offen sc1 lds\n"
    "s_sub_u32 s75, s75, 1\n"
    "s_cselect_b32 s79, s78, s76\n"
    "s_add_u32 s69, s69, s79\n"
    "LOOP%=:\n"
    "s_waitcnt vmcnt(13) lgkmcnt(6)\n"
    "v_mfma_f32_16x16x32_bf16 a[0:3], v[96:99], v[32:35], a[0:3]\n"
    "s_add_u32 s70, s69, 0\n"
    "v_mfma_f32_16x16x32_bf16 a[4:7], v[100:103], v[32:35], a[4:7]\n"
    "s_add_u32 m0, %[mb], 8512\n"
    "v_mfma_f32_16x16x32_bf16 a[8:11], v[104:107], v[32:35], a[8:11]\n"
    "buffer_load_dwordx4 %[vW], s[64:67], s70 offen sc1 lds\n"
    "v_mfma_f32_16x16x32_bf16 a[12:15], v[108:111], v[32:35], a[12:15]\n"
    "ds_read_b128 v[32:35], %[vA1] offset:16896\n"
    "v_mfma_f32_16x16x32_bf16 a[16:19], v[96:99], v[36:39], a[16:19]\n"
    "v_mfma_f32_16x16x32_bf16 a[20:23], v[100:103], v[36:39], a[20:23]\n"
    "buffer_load_dwordx4 v[80:83], %[vW], s[60:63], s68 offen offset:0 sc1\n"
    "v_mfma_f32_16x16x32_bf16 a[24:27], v[104:107], v[36:39], a[24:27]\n"
    "v_mfma_f32_16x16x32_bf16 a[28:31], v[108:111], v[36:39], a[28:31]\n"
    "ds_read_b128 v[36:39], %[vA1] offset:17920\n"
    "s_waitcnt lgkmcnt(6)\n"
    "v_mfma_f32_16x16x32_bf16 a[32:35], v[96:99], v[40:43], a[32:35]\n"
    "v_mfma_f32_16x16x32_bf16 a[36:39], v[100:103], v[40:43], a[36:39]\n"
    "v_mfma_f32_16x16x32_bf16 a[40:43], v[104:107], v[40:43], a[40:43]\n"
    "s_add_u32 s70, s69, 1024\n"
    "v_mfma_f32_16x16x32_bf16 a[44:47], v[108:111], v[40:43], a[44:47]\n"
    "ds_read_b128 v[40:43], %[vA1] offset:18944\n"
    "v_mfma_f32_16x16x32_bf16 a[48:51], v[96:99], v[44:47], a[48:51]\n"
    "s_add_u32 m0, %[mb], 9536\n"
    "v_mfma_f32_16x16x32_bf16 a[52:55], v[100:103], v[44:47], a[52:55]\n"
    "buffer_load_dwordx4 %[vW], s[64:67], s70 offen sc1 lds\n"
    "v_mfma_f32_16x16x32_bf16 a[56:59], v[104:107], v[44:47], a[56:59]\n"
    "buffer_load_dwordx4 v[84:87], %[vW], s[60:63], s68 offen offset:1024 sc1\n"
    "v_mfma_f32_16x16x32_bf16 a[60:63], v[108:111], v[44:47], a[60:63]\n"
    "ds_read_b128 v[44:47], %[vA1] offset:19968\n"
    "s_waitcnt lgkmcnt(6)\n"
    "v_mfma_f32_16x16x32_bf16 a[64:67], v[96:99], v[48:51], a[64:67]\n"
    "v_mfma_f32_16x16x32_bf16 a[68:71], v[100:103], v[48:51], a[68:71]\n"
    "v_mfma_f32_16x16x32_bf16 a[72:75], v[104:107], v[48:51], a[72:75]\n"
    "v_mfma_f32_16x16x32_bf16 a[76:79], v[108:111], v[48:51], a[76:79]\n"
    "ds_read_b128 v[48:51], %[vA1] offset:21120\n"
    "v_mfma_f32_16x16x32_bf16 a[80:83], v[96:99], v[52:55], a[80:83]\n"
    "s_add_u32 s70, s69, 2048\n"
    "v_mfma_f32_16x16x32_bf16 a[84:87], v[100:103], v[52:55], a[84:87]\n"
    "s_add_u32 m0, %[mb], 10560\n"
    "v_mfma_f32_16x16x32_bf16 a[88:91], v[104:107], v[52:55], a[88:91]\n"
    "buffer_load_dwordx4 %[vW], s[64:67], s70 offen sc1 lds\n"
    "v_mfma_f32_16x16x32_bf16 a[92:95], v[108:111], v[52:55], a[92:95]\n"
    "ds_read_b128 v[52:55], %[vA1] offset:22144\n"
    "s_waitcnt lgkmcnt(6)\n"
    "v_mfma_f32_16x16x32_bf16 a[96:99], v[96:99], v[56:59], a[96:99]\n"
    "buffer_load_dwordx4 v[88:91], %[vW], s[60:63], s68 offen offset:2048 sc1\n"
    "v_mfma_f32_16x16x32_bf16 a[100:103], v[100:103], v[56:59], a[100:103]\n"
    "v_mfma_f32_16x16x32_bf16 a[104:107], v[104:107], v[56:59], a[104:107]\n"
    "buffer_load_dwordx4 v[92:95], %[vW], s[60:63], s68 offen offset:3072 sc1\n"
    "v_mfma_f32_16x16x32_bf16 a[108:111], v[108:111], v[56:59], a[108:111]\n"
    "ds_read_b128 v[56:59], %[vA1] offset:23168\n"
    "v_mfma_f32_16x16x32_bf16 a[112:115], v[96:99], v[60:63], a[112:115]\n"
    "s_sub_u32 s74, s74, 1\n"
    "s_cselect_b32 s73, s77, s76\n"
    "v_mfma_f32_16x16x32_bf16 a[116:119], v[100:103], v[60:63], a[116:119]\n"
    "s_add_u32 s68, s68, s73\n"
    "v_mfma_f32_16x16x32_bf16 a[120:123], v[104:107], v[60:63], a[120:123]\n"
    "v_mfma_f32_16x16x32_bf16 a[124:127], v[108:111], v[60:63], a[124:127]\n"
    "ds_read_b128 v[60:63], %[vA1] offset:24192\n"
    "s_waitcnt vmcnt(13) lgkmcnt(6)\n"
    "v_mfma_f32_16x16x32_bf16 a[0:3], v[112:115], v[32:35], a[0:3]\n"
    "s_add_u32 s70, s69, 3072\n"
    "v_mfma_f32_16x16x32_bf16 a[4:7], v[116:119], v[32:35], a[4:7]\n"
    "s_add_u32 m0, %[mb], 11584\n"
    "v_mfma_f32_16x16x32_bf16 a[8:11], v[120:123], v[32:35], a[8:11]\n"
    "buffer_load_dwordx4 %[vW], s[64:67], s70 offen sc1 lds\n"
    "v_mfma_f32_16x16x32_bf16 a[12:15], v[124:127], v[32:35], a[12:15]\n"
    "ds_read_b128 v[32:35], %[vA2] offset:16896\n"
    "v_mfma_f32_16x16x32_bf16 a[16:19], v[112:115], v[36:39], a[16:19]\n"
    "v_mfma_f32_16x16x32_bf16 a[20:23], v[116:119], v[36:39], a[20:23]\n"
    "buffer_load_dwordx4 v[96:99], %[vW], s[60:63], s68 offen offset:0 sc1\n"
    "v_mfma_f32_16x16x32_bf16 a[24:27], v[120:123], v[36:39], a[24:27]\n"
    "v_mfma_f32_16x16x32_bf16 a[28:31], v[124:127], v[36:39], a[28:31]\n"
    "ds_read_b128 v[36:39], %[vA2] offset:17920\n"
    "s_waitcnt lgkmcnt(6)\n"
    "v_mfma_f32_16x16x32_bf16 a[32:35], v[112:115], v[40:43], a[32:35]\n"
    "v_mfma_f32_16x16x32_bf16 a[36:39], v[116:119], v[40:43], a[36:39]\n"
    "v_mfma_f32_16x16x32_bf16 a[40:43], v[120:123], v[40:43], a[40:43]\n"
    "s_add_u32 s70, s69, 262144\n"
    "v_mfma_f32_16x16x32_bf16 a[44:47], v[124:127], v[40:43], a[44:47]\n"
    "ds_read_b128 v[40:43], %[vA2] offset:18944\n"
    "v_mfma_f32_16x16x32_bf16 a[48:51], v[112:115], v[44:47], a[48:51]\n"
    "s_add_u32 m0, %[mb], 12736\n"
    "v_mfma_f32_16x16x32_bf16 a[52:55], v[116:119], v[44:47], a[52:55]\n"
    "buffer_load_dwordx4 %[vW], s[64:67], s70 offen sc1 lds\n"
    "v_mfma_f32_16x16x32_bf16 a[56:59], v[120:123], v[44:47], a[56:59]\n"
    "buffer_load_dwordx4 v[100:103], %[vW], s[60:63], s68 offen offset:1024 sc1\n"
    "v_mfma_f32_16x16x32_bf16 a[60:63], v[124:127], v[44:47], a[60:63]\n"
    "ds_read_b128 v[44:47], %[vA2] offset:19968\n"
    "s_waitcnt lgkmcnt(6)\n"
    "v_mfma_f32_16x16x32_bf16 a[64:67], v[112:115], v[48:51], a[64:67]\n"
    "v_mfma_f32_16x16x32_bf16 a[68:71], v[116:119], v[48:51], a[68:71]\n"
    "v_mfma_f32_16x16x32_bf16 a[72:75], v[120:123], v[48:51], a[72:75]\n"
    "v_mfma_f32_16x16x32_bf16 a[76:79], v[124:127], v[48:51], a[76:79]\n"
    "ds_read_b128 v[48:51], %[vA2] offset:21120\n"
    "v_mfma_f32_16x16x32_bf16 a[80:83], v[112:115], v[52:55], a[80:83]\n"
    "s_add_u32 s70, s69, 263168\n"
    "v_mfma_f32_16x16x32_bf16 a[84:87], v[116:119], v[52:55], a[84:87]\n"
    "s_add_u32 m0, %[mb], 13760\n"
    "v_mfma_f32_16x16x32_bf16 a[88:91], v[120:123], v[52:55], a[88:91]\n"
    "buffer_load_dwordx4 %[vW], s[64:67], s70 offen sc1 lds\n"
    "v_mfma_f32_16x16x32_bf16 a[92:95], v[124:127], v[52:55], a[92:95]\n"
    "ds_read_b128 v[52:55], %[vA2] offset:22144\n"
    "s_waitcnt lgkmcnt(6)\n"
    "v_mfma_f32_16x16x32_bf16 a[96:99], v[112:115], v[56:59], a[96:99]\n"
    "buffer_load_dwordx4 v[104:107], %[vW], s[60:63], s68 offen offset:2048 sc1\n"
    "v_mfma_f32_16x16x32_bf16 a[100:103], v[116:119], v[56:59], a[100:103]\n"
    "v_mfma_f32_16x16x32_bf16 a[104:107], v[120:123], v[56:59], a[104:107]\n"
    "buffer_load_dwordx4 v[108:111], %[vW], s[60:63], s68 offen offset:3072 sc1\n"
    "v_mfma_f32_16x16x32_bf16 a[108:111], v[124:127], v[56:59], a[108:111]\n"
    "ds_read_b128 v[56:59], %[vA2] offset:23168\n"
    "v_mfma_f32_16x16x32_bf16 a[112:115], v[112:115], v[60:63], a[112:115]\n"
    "s_sub_u32 s74, s74, 1\n"
    "s_cselect_b32 s73, s77, s76\n"
    "v_mfma_f32_16x16x32_bf16 a[116:119], v[116:119], v[60:63], a[116:119]\n"
    "s_add_u32 s68, s68, s73\n"
    "v_mfma_f32_16x16x32_bf16 a[120:123], v[120:123], v[60:63], a[120:123]\n"
    "v_mfma_f32_16x16x32_bf16 a[124:127], v[124:127], v[60:63], a[124:127]\n"
    "ds_read_b128 v[60:63], %[vA2] offset:24192\n"
    "s_waitcnt vmcnt(14) lgkmcnt(6)\n"
    "v_mfma_f32_16x16x32_bf16 a[0:3], v[64:67], v[32:35], a[0:3]\n"
    "s_add_u32 s70, s69, 264192\n"
    "v_mfma_f32_16x16x32_bf16 a[4:7], v[68:71], v[32:35], a[4:7]\n"
    "s_add_u32 m0, %[mb], 14784\n"
    "v_mfma_f32_16x16x32_bf16 a[8:11], v[72:75], v[32:35], a[8:11]\n"
    "buffer_load_dwordx4 %[vW], s[64:67], s70 offen sc1 lds\n"
    "v_mfma_f32_16x16x32_bf16 a[12:15], v[76:79], v[32:35], a[12:15]\n"
    "ds_read_b128 v[32:35], %[vA0] offset:25344\n"
    "v_mfma_f32_16x16x32_bf16 a[16:19], v[64:67], v[36:39], a[16:19]\n"
    "v_mfma_f32_16x16x32_bf16 a[20:23], v[68:71], v[36:39], a[20:23]\n"
    "buffer_load_dwordx4 v[112:115], %[vW], s[60:63], s68 offen offset:0 sc1\n"
    "v_mfma_f32_16x16x32_bf16 a[24:27], v[72:75], v[36:39], a[24:27]\n"
    "v_mfma_f32_16x16x32_bf16 a[28:31], v[76:79], v[36:39], a[28:31]\n"
    "ds_read_b128 v[36:39], %[vA0] offset:26368\n"
    "s_waitcnt lgkmcnt(6)\n"
    "v_mfma_f32_16x16x32_bf16 a[32:35], v[64:67], v[40:43], a[32:35]\n"
    "v_mfma_f32_16x16x32_bf16 a[36:39], v[68:71], v[40:43], a[36:39]\n"
    "v_mfma_f32_16x16x32_bf16 a[40:43], v[72:75], v[40:43], a[40:43]\n"
    "v_mfma_f32_16x16x32_bf16 a[44:47], v[76:79], v[40:43], a[44:47]\n"
    "ds_read_b128 v[40:43], %[vA0] offset:27392\n"
    "v_mfma_f32_16x16x32_bf16 a[48:51], v[64:67], v[44:47], a[48:51]\n"
    "v_mfma_f32_16x16x32_bf16 a[52:55], v[68:71], v[44:47], a[52:55]\n"
    "buffer_load_dwordx4 v[116:119], %[vW], s[60:63], s68 offen offset:1024 sc1\n"
    "v_mfma_f32_16x16x32_bf16 a[56:59], v[72:75], v[44:47], a[56:59]\n"
    "v_mfma_f32_16x16x32_bf16 a[60:63], v[76:79], v[44:47], a[60:63]\n"
    "ds_read_b128 v[44:47], %[vA0] offset:28416\n"
    "s_waitcnt lgkmcnt(6)\n"
    "v_mfma_f32_16x16x32_bf16 a[64:67], v[64:67], v[48:51], a[64:67]\n"
    "s_add_u32 s70, s69, 265216\n"
    "v_mfma_f32_16x16x32_bf16 a[68:71], v[68:71], v[48:51], a[68:71]\n"
    "s_add_u32 m0, %[mb], 15808\n"
    "v_mfma_f32_16x16x32_bf16 a[72:75], v[72:75], v[48:51], a[72:75]\n"
    "buffer_load_dwordx4 %[vW], s[64:67], s70 offen sc1 lds\n"
    "v_mfma_f32_16x16x32_bf16 a[76:79], v[76:79], v[48:51], a[76:79]\n"
    "ds_read_b128 v[48:51], %[vA0] offset:29568\n"
    "v_mfma_f32_16x16x32_bf16 a[80:83], v[64:67], v[52:55], a[80:83]\n"
    "s_sub_u32 s75, s75, 1\n"
    "s_cselect_b32 s79, s78, s76\n"
    "v_mfma_f32_16x16x32_bf16 a[84:87], v[68:71], v[52:55], a[84:87]\n"
    "buffer_load_dwordx4 v[120:123], %[vW], s[60:63], s68 offen offset:2048 sc1\n"
    "v_mfma_f32_16x16x32_bf16 a[88:91], v[72:75], v[52:55], a[88:91]\n"
    "s_add_u32 s69, s69, s79\n"
    "v_mfma_f32_16x16x32_bf16 a[92:95], v[76:79], v[52:55], a[92:95]\n"
    "ds_read_b128 v[52:55], %[vA0] offset:30592\n"
    "s_waitcnt lgkmcnt(6)\n"
    "v_mfma_f32_16x16x32_bf16 a[96:99], v[64:67], v[56:59], a[96:99]\n"
    "v_mfma_f32_16x16x32_bf16 a[100:103], v[68:71], v[56:59], a[100:103]\n"
    "v_mfma_f32_16x16x32_bf16 a[104:107], v[72:75], v[56:59], a[104:107]\n"
    "buffer_load_dwordx4 v[124:127], %[vW], s[60:63], s68 offen offset:3072 sc1\n"
    "v_mfma_f32_16x16x32_bf16 a[108:111], v[76:79], v[56:59], a[108:111]\n"
    "ds_read_b128 v[56:59], %[vA0] offset:31616\n"
    "v_mfma_f32_16x16x32_bf16 a[112:115], v[64:67], v[60:63], a[112:115]\n"
    "s_sub_u32 s74, s74, 1\n"
    "s_cselect_b32 s73, s77, s76\n"
    "v_mfma_f32_16x16x32_bf16 a[116:119], v[68:71], v[60:63], a[116:119]\n"
    "s_add_u32 s68, s68, s73\n"
    "v_mfma_f32_16x16x32_bf16 a[120:123], v[72:75], v[60:63], a[120:123]\n"
    "v_mfma_f32_16x16x32_bf16 a[124:127], v[76:79], v[60:63], a[124:127]\n"
    "ds_read_b128 v[60:63], %[vA0] offset:32640\n"
    "s_waitcnt vmcnt(13) lgkmcnt(6)\n"
    "v_mfma_f32_16x16x32_bf16 a[0:3], v[80:83], v[32:35], a[0:3]\n"
    "s_add_u32 s70, s69, 0\n"
    "v_mfma_f32_16x16x32_bf16 a[4:7], v[84:87], v[32:35], a[4:7]\n"
    "s_add_u32 m0, %[mb], 16960\n"
    "v_mfma_f32_16x16x32_bf16 a[8:11], v[88:91], v[32:35], a[8:11]\n"
    "buffer_load_dwordx4 %[vW], s[64:67], s70 offen sc1 lds\n"
    "v_mfma_f32_16x16x32_bf16 a[12:15], v[92:95], v[32:35], a[12:15]\n"
    "ds_read_b128 v[32:35], %[vA1] offset:25344\n"
    "v_mfma_f32_16x16x32_bf16 a[16:19], v[80:83], v[36:39], a[16:19]\n"
    "v_mfma_f32_16x16x32_bf16 a[20:23], v[84:87], v[36:39], a[20:23]\n"
    "buffer_load_dwordx4 v[64:67], %[vW], s[60:63], s68 offen offset:0 sc1\n"
    "v_mfma_f32_16x16x32_bf16 a[24:27], v[88:91], v[36:39], a[24:27]\n"
    "v_mfma_f32_16x16x32_bf16 a[28:31], v[92:95], v[36:39], a[28:31]\n"
    "ds_read_b128 v[36:39], %[vA1] offset:26368\n"
    "s_waitcnt lgkmcnt(6)\n"
    "v_mfma_f32_16x16x32_bf16 a[32:35], v[80:83], v[40:43], a[32:35]\n"
    "v_mfma_f32_16x16x32_bf16 a[36:39], v[84:87], v[40:43], a[36:39]\n"
    "v_mfma_f32_16x16x32_bf16 a[40:43], v[88:91], v[40:43], a[40:43]\n"
    "s_add_u32 s70, s69, 1024\n"
    "v_mfma_f32_16x16x32_bf16 a[44:47], v[92:95], v[40:43], a[44:47]\n"
    "ds_read_b128 v[40:43], %[vA1] offset:27392\n"
    "v_mfma_f32_16x16x32_bf16 a[48:51], v[80:83], v[44:47], a[48:51]\n"
    "s_add_u32 m0, %[mb], 17984\n"
    "v_mfma_f32_16x16x32_bf16 a[52:55], v[84:87], v[44:47], a[52:55]\n"
    "buffer_load_dwordx4 %[vW], s[64:67], s70 offen sc1 lds\n"
    "v_mfma_f32_16x16x32_bf16 a[56:59], v[88:91], v[44:47], a[56:59]\n"
    "buffer_load_dwordx4 v[68:71], %[vW], s[60:63], s68 offen offset:1024 sc1\n"
    "v_mfma_f32_16x16x32_bf16 a[60:63], v[92:95], v[44:47], a[60:63]\n"
    "ds_read_b128 v[44:47], %[vA1] offset:28416\n"
    "s_waitcnt lgkmcnt(6)\n"
    "v_mfma_f32_16x16x32_bf16 a[64:67], v[80:83], v[48:51], a[64:67]\n"
    "v_mfma_f32_16x16x32_bf16 a[68:71], v[84:87], v[48:51], a[68:71]\n"
    "v_mfma_f32_16x16x32_bf16 a[72:75], v[88:91], v[48:51], a[72:75]\n"
    "v_mfma_f32_16x16x32_bf16 a[76:79], v[92:95], v[48:51], a[76:79]\n"
    "ds_read_b128 v[48:51], %[vA1] offset:29568\n"
    "v_mfma_f32_16x16x32_bf16 a[80:83], v[80:83], v[52:55], a[80:83]\n"
    "s_add_u32 s70, s69, 2048\n"
    "v_mfma_f32_16x16x32_bf16 a[84:87], v[84:87], v[52:55], a[84:87]\n"
    "s_add_u32 m0, %[mb], 19008\n"
    "v_mfma_f32_16x16x32_bf16 a[88:91], v[88:91], v[52:55], a[88:91]\n"
    "buffer_load_dwordx4 %[vW], s[64:67], s70 offen sc1 lds\n"
    "v_mfma_f32_16x16x32_bf16 a[92:95], v[92:95], v[52:55], a[92:95]\n"
    "ds_read_b128 v[52:55], %[vA1] offset:30592\n"
    "s_waitcnt lgkmcnt(6)\n"
    "v_mfma_f32_16x16x32_bf16 a[96:99], v[80:83], v[56:59], a[96:99]\n"
    "buffer_load_dwordx4 v[72:75], %[vW], s[60:63], s68 offen offset:2048 sc1\n"
    "v_mfma_f32_16x16x32_bf16 a[100:103], v[84:87], v[56:59], a[100:103]\n"
    "v_mfma_f32_16x16x32_bf16 a[104:107], v[88:91], v[56:59], a[104:107]\n"
    "buffer_load_dwordx4 v[76:79], %[vW], s[60:63], s68 offen offset:3072 sc1\n"
    "v_mfma_f32_16x16x32_bf16 a[108:111], v[92:95], v[56:59], a[108:111]\n"
    "ds_read_b128 v[56:59], %[vA1] offset:31616\n"
    "v_mfma_f32_16x16x32_bf16 a[112:115], v[80:83], v[60:63], a[112:115]\n"
    "s_sub_u32 s74, s74, 1\n"
    "s_cselect_b32 s73, s77, s76\n"
    "v_mfma_f32_16x16x32_bf16 a[116:119], v[84:87], v[60:63], a[116:119]\n"
    "s_add_u32 s68, s68, s73\n"
    "v_mfma_f32_16x16x32_bf16 a[120:123], v[88:91], v[60:63], a[120:123]\n"
    "v_mfma_f32_16x16x32_bf16 a[124:127], v[92:95], v[60:63], a[124:127]\n"
    "ds_read_b128 v[60:63], %[vA1] offset:32640\n"
    "s_waitcnt vmcnt(13) lgkmcnt(6)\n"
    "v_mfma_f32_16x16x32_bf16 a[0:3], v[96:99], v[32:35], a[0:3]\n"
    "s_add_u32 s70, s69, 3072\n"
    "v_mfma_f32_16x16x32_bf16 a[4:7], v[100:103], v[32:35], a[4:7]\n"
    "s_add_u32 m0, %[mb], 20032\n"
    "v_mfma_f32_16x16x32_bf16 a[8:11], v[104:107], v[32:35], a[8:11]\n"
    "buffer_load_dwordx4 %[vW], s[64:67], s70 offen sc1 lds\n"
    "v_mfma_f32_16x16x32_bf16 a[12:15], v[108:111], v[32:35], a[12:15]\n"
    "ds_read_b128 v[32:35], %[vA2] offset:25344\n"
    "v_mfma_f32_16x16x32_bf16 a[16:19], v[96:99], v[36:39], a[16:19]\n"
    "v_mfma_f32_16x16x32_bf16 a[20:23], v[100:103], v[36:39], a[20:23]\n"
    "buffer_load_dwordx4 v[80:83], %[vW], s[60:63], s68 offen offset:0 sc1\n"
    "v_mfma_f32_16x16x32_bf16 a[24:27], v[104:107], v[36:39], a[24:27]\n"
    "v_mfma_f32_16x16x32_bf16 a[28:31], v[108:111], v[36:39], a[28:31]\n"
    "ds_read_b128 v[36:39], %[vA2] offset:26368\n"
    "s_waitcnt lgkmcnt(6)\n"
    "v_mfma_f32_16x16x32_bf16 a[32:35], v[96:99], v[40:43], a[32:35]\n"
    "v_mfma_f32_16x16x32_bf16 a[36:39], v[100:103], v[40:43], a[36:39]\n"
    "v_mfma_f32_16x16x32_bf16 a[40:43], v[104:107], v[40:43], a[40:43]\n"
    "s_add_u32 s70, s69, 262144\n"
    "v_mfma_f32_16x16x32_bf16 a[44:47], v[108:111], v[40:43], a[44:47]\n"
    "ds_read_b128 v[40:43], %[vA2] offset:27392\n"
    "v_mfma_f32_16x16x32_bf16 a[48:51], v[96:99], v[44:47], a[48:51]\n"
    "s_add_u32 m0, %[mb], 21184\n"
    "v_mfma_f32_16x16x32_bf16 a[52:55], v[100:103], v[44:47], a[52:55]\n"
    "buffer_load_dwordx4 %[vW], s[64:67], s70 offen sc1 lds\n"
    "v_mfma_f32_16x16x32_bf16 a[56:59], v[104:107], v[44:47], a[56:59]\n"
    "buffer_load_dwordx4 v[84:87], %[vW], s[60:63], s68 offen offset:1024 sc1\n"
    "v_mfma_f32_16x16x32_bf16 a[60:63], v[108:111], v[44:47], a[60:63]\n"
    "ds_read_b128 v[44:47], %[vA2] offset:28416\n"
    "s_waitcnt lgkmcnt(6)\n"
    "v_mfma_f32_16x16x32_bf16 a[64:67], v[96:99], v[48:51], a[64:67]\n"
    "v_mfma_f32_16x16x32_bf16 a[68:71], v[100:103], v[48:51], a[68:71]\n"
    "v_mfma_f32_16x16x32_bf16 a[72:75], v[104:107], v[48:51], a[72:75]\n"
    "v_mfma_f32_16x16x32_bf16 a[76:79], v[108:111], v[48:51], a[76:79]\n"
    "ds_read_b128 v[48:51], %[vA2] offset:29568\n"
    "v_mfma_f32_16x16x32_bf16 a[80:83], v[96:99], v[52:55], a[80:83]\n"
    "s_add_u32 s70, s69, 263168\n"
    "v_mfma_f32_16x16x32_bf16 a[84:87], v[100:103], v[52:55], a[84:87]\n"
    "s_add_u32 m0, %[mb], 22208\n"
    "v_mfma_f32_16x16x32_bf16 a[88:91], v[104:107], v[52:55], a[88:91]\n"
    "buffer_load_dwordx4 %[vW], s[64:67], s70 offen sc1 lds\n"
    "v_mfma_f32_16x16x32_bf16 a[92:95], v[108:111], v[52:55], a[92:95]\n"
    "ds_read_b128 v[52:55], %[vA2] offset:30592\n"
    "s_waitcnt lgkmcnt(6)\n"
    "v_mfma_f32_16x16x32_bf16 a[96:99], v[96:99], v[56:59], a[96:99]\n"
    "buffer_load_dwordx4 v[88:91], %[vW], s[60:63], s68 offen offset:2048 sc1\n"
    "v_mfma_f32_16x16x32_bf16 a[100:103], v[100:103], v[56:59], a[100:103]\n"
    "v_mfma_f32_16x16x32_bf16 a[104:107], v[104:107], v[56:59], a[104:107]\n"
    "buffer_load_dwordx4 v[92:95], %[vW], s[60:63], s68 offen offset:3072 sc1\n"
    "v_mfma_f32_16x16x32_bf16 a[108:111], v[108:111], v[56:59], a[108:111]\n"
    "ds_read_b128 v[56:59], %[vA2] offset:31616\n"
    "v_mfma_f32_16x16x32_bf16 a[112:115], v[96:99], v[60:63], a[112:115]\n"
    "s_sub_u32 s74, s74, 1\n"
    "s_cselect_b32 s73, s77, s76\n"
    "v_mfma_f32_16x16x32_bf16 a[116:119], v[100:103], v[60:63], a[116:119]\n"
    "s_add_u32 s68, s68, s73\n"
    "v_mfma_f32_16x16x32_bf16 a[120:123], v[104:107], v[60:63], a[120:123]\n"
    "v_mfma_f32_16x16x32_bf16 a[124:127], v[108:111], v[60:63], a[124:127]\n"
    "ds_read_b128 v[60:63], %[vA2] offset:32640\n"
    "s_waitcnt vmcnt(14) lgkmcnt(6)\n"
    "v_mfma_f32_16x16x32_bf16 a[0:3], v[112:115], v[32:35], a[0:3]\n"
    "s_add_u32 s70, s69, 264192\n"
    "v_mfma_f32_16x16x32_bf16 a[4:7], v[116:119], v[32:35], a[4:7]\n"
    "s_add_u32 m0, %[mb], 23232\n"
    "v_mfma_f32_16x16x32_bf16 a[8:11], v[120:123], v[32:35], a[8:11]\n"
    "buffer_load_dwordx4 %[vW], s[64:67], s70 offen sc1 lds\n"
    "v_mfma_f32_16x16x32_bf16 a[12:15], v[124:127], v[32:35], a[12:15]\n"
    "ds_read_b128 v[32:35], %[vA0] offset:0\n"
    "v_mfma_f32_16x16x32_bf16 a[16:19], v[112:115], v[36:39], a[16:19]\n"
    "v_mfma_f32_16x16x32_bf16 a[20:23], v[116:119], v[36:39], a[20:23]\n"
    "buffer_load_dwordx4 v[96:99], %[vW], s[60:63], s68 offen offset:0 sc1\n"
    "v_mfma_f32_16x16x32_bf16 a[24:27], v[120:123], v[36:39], a[24:27]\n"
    "v_mfma_f32_16x16x32_bf16 a[28:31], v[124:127], v[36:39], a[28:31]\n"
    "ds_read_b128 v[36:39], %[vA0] offset:1024\n"
    "s_waitcnt lgkmcnt(6)\n"
    "v_mfma_f32_16x16x32_bf16 a[32:35], v[112:115], v[40:43], a[32:35]\n"
    "v_mfma_f32_16x16x32_bf16 a[36:39], v[116:119], v[40:43], a[36:39]\n"
    "v_mfma_f32_16x16x32_bf16 a[40:43], v[120:123], v[40:43], a[40:43]\n"
    "v_mfma_f32_16x16x32_bf16 a[44:47], v[124:127], v[40:43], a[44:47]\n"
    "ds_read_b128 v[40:43], %[vA0] offset:2048\n"
    "v_mfma_f32_16x16x32_bf16 a[48:51], v[112:115], v[44:47], a[48:51]\n"
    "v_mfma_f32_16x16x32_bf16 a[52:55], v[116:119], v[44:47], a[52:55]\n"
    "buffer_load_dwordx4 v[100:103], %[vW], s[60:63], s68 offen offset:1024 sc1\n"
    "v_mfma_f32_16x16x32_bf16 a[56:59], v[120:123], v[44:47], a[56:59]\n"
    "v_mfma_f32_16x16x32_bf16 a[60:63], v[124:127], v[44:47], a[60:63]\n"
    "ds_read_b128 v[44:47], %[vA0] offset:3072\n"
    "s_waitcnt lgkmcnt(6)\n"
    "v_mfma_f32_16x16x32_bf16 a[64:67], v[112:115], v[48:51], a[64:67]\n"
    "s_add_u32 s70, s69, 265216\n"
    "v_mfma_f32_16x16x32_bf16 a[68:71], v[116:119], v[48:51], a[68:71]\n"
    "s_add_u32 m0, %[mb], 24256\n"
    "v_mfma_f32_16x16x32_bf16 a[72:75], v[120:123], v[48:51], a[72:75]\n"
    "buffer_load_dwordx4 %[vW], s[64:67], s70 offen sc1 lds\n"
    "v_mfma_f32_16x16x32_bf16 a[76:79], v[124:127], v[48:51], a[76:79]\n"
    "ds_read_b128 v[48:51], %[vA0] offset:4224\n"
    "v_mfma_f32_16x16x32_bf16 a[80:83], v[112:115], v[52:55], a[80:83]\n"
    "s_sub_u32 s75, s75, 1\n"
    "s_cselect_b32 s79, s78, s76\n"
    "v_mfma_f32_16x16x32_bf16 a[84:87], v[116:119], v[52:55], a[84:87]\n"
    "buffer_load_dwordx4 v[104:107], %[vW], s[60:63], s68 offen offset:2048 sc1\n"
    "v_mfma_f32_16x16x32_bf16 a[88:91], v[120:123], v[52:55], a[88:91]\n"
    "s_add_u32 s69, s69, s79\n"
    "v_mfma_f32_16x16x32_bf16 a[92:95], v[124:127], v[52:55], a[92:95]\n"
    "ds_read_b128 v[52:55], %[vA0] offset:5248\n"
    "s_waitcnt lgkmcnt(6)\n"
    "v_mfma_f32_16x16x32_bf16 a[96:99], v[112:115], v[56:59], a[96:99]\n"
    "v_mfma_f32_16x16x32_bf16 a[100:103], v[116:119], v[56:59], a[100:103]\n"
    "v_mfma_f32_16x16x32_bf16 a[104:107], v[120:123], v[56:59], a[104:107]\n"
    "buffer_load_dwordx4 v[108:111], %[vW], s[60:63], s68 offen offset:3072 sc1\n"
    "v_mfma_f32_16x16x32_bf16 a[108:111], v[124:127], v[56:59], a[108:111]\n"
    "ds_read_b128 v[56:59], %[vA0] offset:6272\n"
    "v_mfma_f32_16x16x32_bf16 a[112:115], v[112:115], v[60:63], a[112:115]\n"
    "s_sub_u32 s74, s74, 1\n"
    "s_cselect_b32 s73, s77, s76\n"
    "v_mfma_f32_16x16x32_bf16 a[116:119], v[116:119], v[60:63], a[116:119]\n"
    "s_add_u32 s68, s68, s73\n"
    "v_mfma_f32_16x16x32_bf16 a[120:123], v[120:123], v[60:63], a[120:123]\n"
    "v_mfma_f32_16x16x32_bf16 a[124:127], v[124:127], v[60:63], a[124:127]\n"
    "ds_read_b128 v[60:63], %[vA0] offset:7296\n"
    "s_waitcnt vmcnt(13) lgkmcnt(6)\n"
    "v_mfma_f32_16x16x32_bf16 a[0:3], v[64:67], v[32:35], a[0:3]\n"
    "s_add_u32 s70, s69, 0\n"
    "v_mfma_f32_16x16x32_bf16 a[4:7], v[68:71], v[32:35], a[4:7]\n"
    "s_add_u32 m0, %[mb], 25408\n"
    "v_mfma_f32_16x16x32_bf16 a[8:11], v[72:75], v[32:35], a[8:11]\n"
    "buffer_load_dwordx4 %[vW], s[64:67], s70 offen sc1 lds\n"
    "v_mfma_f32_16x16x32_bf16 a[12:15], v[76:79], v[32:35], a[12:15]\n"
    "ds_read_b128 v[32:35], %[vA1] offset:0\n"
    "v_mfma_f32_16x16x32_bf16 a[16:19], v[64:67], v[36:39], a[16:19]\n"
    "v_mfma_f32_16x16x32_bf16 a[20:23], v[68:71], v[36:39], a[20:23]\n"
    "buffer_load_dwordx4 v[112:115], %[vW], s[60:63], s68 offen offset:0 sc1\n"
    "v_mfma_f32_16x16x32_bf16 a[24:27], v[72:75], v[36:39], a[24:27]\n"
    "v_mfma_f32_16x16x32_bf16 a[28:31], v[76:79], v[36:39], a[28:31]\n"
    "ds_read_b128 v[36:39], %[vA1] offset:1024\n"
    "s_waitcnt lgkmcnt(6)\n"
    "v_mfma_f32_16x16x32_bf16 a[32:35], v[64:67], v[40:43], a[32:35]\n"
    "v_mfma_f32_16x16x32_bf16 a[36:39], v[68:71], v[40:43], a[36:39]\n"
    "v_mfma_f32_16x16x32_bf16 a[40:43], v[72:75], v[40:43], a[40:43]\n"
    "s_add_u32 s70, s69, 1024\n"
    "v_mfma_f32_16x16x32_bf16 a[44:47], v[76:79], v[40:43], a[44:47]\n"
    "ds_read_b128 v[40:43], %[vA1] offset:2048\n"
    "v_mfma_f32_16x16x32_bf16 a[48:51], v[64:67], v[44:47], a[48:51]\n"
    "s_add_u32 m0, %[mb], 26432\n"
    "v_mfma_f32_16x16x32_bf16 a[52:55], v[68:71], v[44:47], a[52:55]\n"
    "buffer_load_dwordx4 %[vW], s[64:67], s70 offen sc1 lds\n"
    "v_mfma_f32_16x16x32_bf16 a[56:59], v[72:75], v[44:47], a[56:59]\n"
    "buffer_load_dwordx4 v[116:119], %[vW], s[60:63], s68 offen offset:1024 sc1\n"
    "v_mfma_f32_16x16x32_bf16 a[60:63], v[76:79], v[44:47], a[60:63]\n"
    "ds_read_b128 v[44:47], %[vA1] offset:3072\n"
    "s_waitcnt lgkmcnt(6)\n"
    "v_mfma_f32_16x16x32_bf16 a[64:67], v[64:67], v[48:51], a[64:67]\n"
    "v_mfma_f32_16x16x32_bf16 a[68:71], v[68:71], v[48:51], a[68:71]\n"
    "v_mfma_f32_16x16x32_bf16 a[72:75], v[72:75], v[48:51], a[72:75]\n"
    "v_mfma_f32_16x16x32_bf16 a[76:79], v[76:79], v[48:51], a[76:79]\n"
    "ds_read_b128 v[48:51], %[vA1] offset:4224\n"
    "v_mfma_f32_16x16x32_bf16 a[80:83], v[64:67], v[52:55], a[80:83]\n"
    "s_add_u32 s70, s69, 2048\n"
    "v_mfma_f32_16x16x32_bf16 a[84:87], v[68:71], v[52:55], a[84:87]\n"
    "s_add_u32 m0, %[mb], 27456\n"
    "v_mfma_f32_16x16x32_bf16 a[88:91], v[72:75], v[52:55], a[88:91]\n"
    "buffer_load_dwordx4 %[vW], s[64:67], s70 offen sc1 lds\n"
    "v_mfma_f32_16x16x32_bf16 a[92:95], v[76:79], v[52:55], a[92:95]\n"
    "ds_read_b128 v[52:55], %[vA1] offset:5248\n"
    "s_waitcnt lgkmcnt(6)\n"
    "v_mfma_f32_16x16x32_bf16 a[96:99], v[64:67], v[56:59], a[96:99]\n"
    "buffer_load_dwordx4 v[120:123], %[vW], s[60:63], s68 offen offset:2048 sc1\n"
    "v_mfma_f32_16x16x32_bf16 a[100:103], v[68:71], v[56:59], a[100:103]\n"
    "v_mfma_f32_16x16x32_bf16 a[104:107], v[72:75], v[56:59], a[104:107]\n"
    "buffer_load_dwordx4 v[124:127], %[vW], s[60:63], s68 offen offset:3072 sc1\n"
    "v_mfma_f32_16x16x32_bf16 a[108:111], v[76:79], v[56:59], a[108:111]\n"
    "ds_read_b128 v[56:59], %[vA1] offset:6272\n"
    "v_mfma_f32_16x16x32_bf16 a[112:115], v[64:67], v[60:63], a[112:115]\n"
    "s_sub_u32 s74, s74, 1\n"
    "s_cselect_b32 s73, s77, s76\n"
    "v_mfma_f32_16x16x32_bf16 a[116:119], v[68:71], v[60:63], a[116:119]\n"
    "s_add_u32 s68, s68, s73\n"
    "v_mfma_f32_16x16x32_bf16 a[120:123], v[72:75], v[60:63], a[120:123]\n"
    "v_mfma_f32_16x16x32_bf16 a[124:127], v[76:79], v[60:63], a[124:127]\n"
    "ds_read_b128 v[60:63], %[vA1] offset:7296\n"
    "s_waitcnt vmcnt(13) lgkmcnt(6)\n"
    "v_mfma_f32_16x16x32_bf16 a[0:3], v[80:83], v[32:35], a[0:3]\n"
    "s_add_u32 s70, s69, 3072\n"
    "v_mfma_f32_16x16x32_bf16 a[4:7], v[84:87], v[32:35], a[4:7]\n"
    "s_add_u32 m0, %[mb], 28480\n"
    "v_mfma_f32_16x16x32_bf16 a[8:11], v[88:91], v[32:35], a[8:11]\n"
    "buffer_load_dwordx4 %[vW], s[64:67], s70 offen sc1 lds\n"
    "v_mfma_f32_16x16x32_bf16 a[12:15], v[92:95], v[32:35], a[12:15]\n"
    "ds_read_b128 v[32:35], %[vA2] offset:0\n"
    "v_mfma_f32_16x16x32_bf16 a[16:19], v[80:83], v[36:39], a[16:19]\n"
    "v_mfma_f32_16x16x32_bf16 a[20:23], v[84:87], v[36:39], a[20:23]\n"
    "buffer_load_dwordx4 v[64:67], %[vW], s[60:63], s68 offen offset:0 sc1\n"
    "v_mfma_f32_16x16x32_bf16 a[24:27], v[88:91], v[36:39], a[24:27]\n"
    "v_mfma_f32_16x16x32_bf16 a[28:31], v[92:95], v[36:39], a[28:31]\n"
    "ds_read_b128 v[36:39], %[vA2] offset:1024\n"
    "s_waitcnt lgkmcnt(6)\n"
    "v_mfma_f32_16x16x32_bf16 a[32:35], v[80:83], v[40:43], a[32:35]\n"
    "v_mfma_f32_16x16x32_bf16 a[36:39], v[84:87], v[40:43], a[36:39]\n"
    "v_mfma_f32_16x16x32_bf16 a[40:43], v[88:91], v[40:43], a[40:43]\n"
    "s_add_u32 s70, s69, 262144\n"
    "v_mfma_f32_16x16x32_bf16 a[44:47], v[92:95], v[40:43], a[44:47]\n"
    "ds_read_b128 v[40:43], %[vA2] offset:2048\n"
    "v_mfma_f32_16x16x32_bf16 a[48:51], v[80:83], v[44:47], a[48:51]\n"
    "s_add_u32 m0, %[mb], 29632\n"
    "v_mfma_f32_16x16x32_bf16 a[52:55], v[84:87], v[44:47], a[52:55]\n"
    "buffer_load_dwordx4 %[vW], s[64:67], s70 offen sc1 lds\n"
    "v_mfma_f32_16x16x32_bf16 a[56:59], v[88:91], v[44:47], a[56:59]\n"
    "buffer_load_dwordx4 v[68:71], %[vW], s[60:63], s68 offen offset:1024 sc1\n"
    "v_mfma_f32_16x16x32_bf16 a[60:63], v[92:95], v[44:47], a[60:63]\n"
    "ds_read_b128 v[44:47], %[vA2] offset:3072\n"
    "s_waitcnt lgkmcnt(6)\n"
    "v_mfma_f32_16x16x32_bf16 a[64:67], v[80:83], v[48:51], a[64:67]\n"
    "v_mfma_f32_16x16x32_bf16 a[68:71], v[84:87], v[48:51], a[68:71]\n"
    "v_mfma_f32_16x16x32_bf16 a[72:75], v[88:91], v[48:51], a[72:75]\n"
    "v_mfma_f32_16x16x32_bf16 a[76:79], v[92:95], v[48:51], a[76:79]\n"
    "ds_read_b128 v[48:51], %[vA2] offset:4224\n"
    "v_mfma_f32_16x16x32_bf16 a[80:83], v[80:83], v[52:55], a[80:83]\n"
    "s_add_u32 s70, s69, 263168\n"
    "v_mfma_f32_16x16x32_bf16 a[84:87], v[84:87], v[52:55], a[84:87]\n"
    "s_add_u32 m0, %[mb], 30656\n"
    "v_mfma_f32_16x16x32_bf16 a[88:91], v[88:91], v[52:55], a[88:91]\n"
    "buffer_load_dwordx4 %[vW], s[64:67], s70 offen sc1 lds\n"
    "v_mfma_f32_16x16x32_bf16 a[92:95], v[92:95], v[52:55], a[92:95]\n"
    "ds_read_b128 v[52:55], %[vA2] offset:5248\n"
    "s_waitcnt lgkmcnt(6)\n"
    "v_mfma_f32_16x16x32_bf16 a[96:99], v[80:83], v[56:59], a[96:99]\n"
    "buffer_load_dwordx4 v[72:75], %[vW], s[60:63], s68 offen offset:2048 sc1\n"
    "v_mfma_f32_16x16x32_bf16 a[100:103], v[84:87], v[56:59], a[100:103]\n"
    "v_mfma_f32_16x16x32_bf16 a[104:107], v[88:91], v[56:59], a[104:107]\n"
    "buffer_load_dwordx4 v[76:79], %[vW], s[60:63], s68 offen offset:3072 sc1\n"
    "v_mfma_f32_16x16x32_bf16 a[108:111], v[92:95], v[56:59], a[108:111]\n"
    "ds_read_b128 v[56:59], %[vA2] offset:6272\n"
    "v_mfma_f32_16x16x32_bf16 a[112:115], v[80:83], v[60:63], a[112:115]\n"
    "s_sub_u32 s74, s74, 1\n"
    "s_cselect_b32 s73, s77, s76\n"
    "v_mfma_f32_16x16x32_bf16 a[116:119], v[84:87], v[60:63], a[116:119]\n"
    "s_add_u32 s68, s68, s73\n"
    "v_mfma_f32_16x16x32_bf16 a[120:123], v[88:91], v[60:63], a[120:123]\n"
    "v_mfma_f32_16x16x32_bf16 a[124:127], v[92:95], v[60:63], a[124:127]\n"
    "ds_read_b128 v[60:63], %[vA2] offset:7296\n"
    "s_waitcnt vmcnt(14) lgkmcnt(6)\n"
    "v_mfma_f32_16x16x32_bf16 a[0:3], v[96:99], v[32:35], a[0:3]\n"
    "s_add_u32 s70, s69, 264192\n"
    "v_mfma_f32_16x16x32_bf16 a[4:7], v[100:103], v[32:35], a[4:7]\n"
    "s_add_u32 m0, %[mb], 31680\n"
    "v_mfma_f32_16x16x32_bf16 a[8:11], v[104:107], v[32:35], a[8:11]\n"
    "buffer_load_dwordx4 %[vW], s[64:67], s70 offen sc1 lds\n"
    "v_mfma_f32_16x16x32_bf16 a[12:15], v[108:111], v[32:35], a[12:15]\n"
    "ds_read_b128 v[32:35], %[vA0] offset:8448\n"
    "v_mfma_f32_16x16x32_bf16 a[16:19], v[96:99], v[36:39], a[16:19]\n"
    "v_mfma_f32_16x16x32_bf16 a[20:23], v[100:103], v[36:39], a[20:23]\n"
    "buffer_load_dwordx4 v[80:83], %[vW], s[60:63], s68 offen offset:0 sc1\n"
    "v_mfma_f32_16x16x32_bf16 a[24:27], v[104:107], v[36:39], a[24:27]\n"
    "v_mfma_f32_16x16x32_bf16 a[28:31], v[108:111], v[36:39], a[28:31]\n"
    "ds_read_b128 v[36:39], %[vA0] offset:9472\n"
    "s_waitcnt lgkmcnt(6)\n"
    "v_mfma_f32_16x16x32_bf16 a[32:35], v[96:99], v[40:43], a[32:35]\n"
    "v_mfma_f32_16x16x32_bf16 a[36:39], v[100:103], v[40:43], a[36:39]\n"
    "v_mfma_f32_16x16x32_bf16 a[40:43], v[104:107], v[40:43], a[40:43]\n"
    "v_mfma_f32_16x16x32_bf16 a[44:47], v[108:111], v[40:43], a[44:47]\n"
    "ds_read_b128 v[40:43], %[vA0] offset:10496\n"
    "v_mfma_f32_16x16x32_bf16 a[48:51], v[96:99], v[44:47], a[48:51]\n"
    "v_mfma_f32_16x16x32_bf16 a[52:55], v[100:103], v[44:47], a[52:55]\n"
    "buffer_load_dwordx4 v[84:87], %[vW], s[60:63], s68 offen offset:1024 sc1\n"
    "v_mfma_f32_16x16x32_bf16 a[56:59], v[104:107], v[44:47], a[56:59]\n"
    "v_mfma_f32_16x16x32_bf16 a[60:63], v[108:111], v[44:47], a[60:63]\n"
    "ds_read_b128 v[44:47], %[vA0] offset:11520\n"
    "s_waitcnt lgkmcnt(6)\n"
    "v_mfma_f32_16x16x32_bf16 a[64:67], v[96:99], v[48:51], a[64:67]\n"
    "s_add_u32 s70, s69, 265216\n"
    "v_mfma_f32_16x16x32_bf16 a[68:71], v[100:103], v[48:51], a[68:71]\n"
    "s_add_u32 m0, %[mb], 32704\n"
    "v_mfma_f32_16x16x32_bf16 a[72:75], v[104:107], v[48:51], a[72:75]\n"
    "buffer_load_dwordx4 %[vW], s[64:67], s70 offen sc1 lds\n"
    "v_mfma_f32_16x16x32_bf16 a[76:79], v[108:111], v[48:51], a[76:79]\n"
    "ds_read_b128 v[48:51], %[vA0] offset:12672\n"
    "v_mfma_f32_16x16x32_bf16 a[80:83], v[96:99], v[52:55], a[80:83]\n"
    "s_sub_u32 s75, s75, 1\n"
    "s_cselect_b32 s79, s78, s76\n"
    "v_mfma_f32_16x16x32_bf16 a[84:87], v[100:103], v[52:55], a[84:87]\n"
    "buffer_load_dwordx4 v[88:91], %[vW], s[60:63], s68 offen offset:2048 sc1\n"
    "v_mfma_f32_16x16x32_bf16 a[88:91], v[104:107], v[52:55], a[88:91]\n"
    "s_add_u32 s69, s69, s79\n"
    "v_mfma_f32_16x16x32_bf16 a[92:95], v[108:111], v[52:55], a[92:95]\n"
    "ds_read_b128 v[52:55], %[vA0] offset:13696\n"
    "s_waitcnt lgkmcnt(6)\n"
    "v_mfma_f32_16x16x32_bf16 a[96:99], v[96:99], v[56:59], a[96:99]\n"
    "v_mfma_f32_16x16x32_bf16 a[100:103], v[100:103], v[56:59], a[100:103]\n"
    "v_mfma_f32_16x16x32_bf16 a[104:107], v[104:107], v[56:59], a[104:107]\n"
    "buffer_load_dwordx4 v[92:95], %[vW], s[60:63], s68 offen offset:3072 sc1\n"
    "v_mfma_f32_16x16x32_bf16 a[108:111], v[108:111], v[56:59], a[108:111]\n"
    "ds_read_b128 v[56:59], %[vA0] offset:14720\n"
    "v_mfma_f32_16x16x32_bf16 a[112:115], v[96:99], v[60:63], a[112:115]\n"
    "s_sub_u32 s74, s74, 1\n"
    "s_cselect_b32 s73, s77, s76\n"
    "v_mfma_f32_16x16x32_bf16 a[116:119], v[100:103], v[60:63], a[116:119]\n"
    "s_add_u32 s68, s68, s73\n"
    "v_mfma_f32_16x16x32_bf16 a[120:123], v[104:107], v[60:63], a[120:123]\n"
    "v_mfma_f32_16x16x32_bf16 a[124:127], v[108:111], v[60:63], a[124:127]\n"
    "ds_read_b128 v[60:63], %[vA0] offset:15744\n"
    "s_cmp_eq_u32 s71, 2\n"
    "s_cselect_b32 s66, 0, 327680\n"
    "s_waitcnt vmcnt(13) lgkmcnt(6)\n"
    "v_mfma_f32_16x16x32_bf16 a[0:3], v[112:115], v[32:35], a[0:3]\n"
    "s_add_u32 s70, s69, 0\n"
    "v_mfma_f32_16x16x32_bf16 a[4:7], v[116:119], v[32:35], a[4:7]\n"
    "s_add_u32 m0, %[mb], 64\n"
    "v_mfma_f32_16x16x32_bf16 a[8:11], v[120:123], v[32:35], a[8:11]\n"
    "buffer_load_dwordx4 %[vW], s[64:67], s70 offen sc1 lds\n"
    "v_mfma_f32_16x16x32_bf16 a[12:15], v[124:127], v[32:35], a[12:15]\n"
    "ds_read_b128 v[32:35], %[vA1] offset:8448\n"
    "v_mfma_f32_16x16x32_bf16 a[16:19], v[112:115], v[36:39], a[16:19]\n"
    "v_mfma_f32_16x16x32_bf16 a[20:23], v[116:119], v[36:39], a[20:23]\n"
    "buffer_load_dwordx4 v[96:99], %[vW], s[60:63], s68 offen offset:0 sc1\n"
    "v_mfma_f32_16x16x32_bf16 a[24:27], v[120:123], v[36:39], a[24:27]\n"
    "v_mfma_f32_16x16x32_bf16 a[28:31], v[124:127], v[36:39], a[28:31]\n"
    "ds_read_b128 v[36:39], %[vA1] offset:9472\n"
    "s_waitcnt lgkmcnt(6)\n"
    "v_mfma_f32_16x16x32_bf16 a[32:35], v[112:115], v[40:43], a[32:35]\n"
    "v_mfma_f32_16x16x32_bf16 a[36:39], v[116:119], v[40:43], a[36:39]\n"
    "v_mfma_f32_16x16x32_bf16 a[40:43], v[120:123], v[40:43], a[40:43]\n"
    "s_add_u32 s70, s69, 1024\n"
    "v_mfma_f32_16x16x32_bf16 a[44:47], v[124:127], v[40:43], a[44:47]\n"
    "ds_read_b128 v[40:43], %[vA1] offset:10496\n"
    "v_mfma_f32_16x16x32_bf16 a[48:51], v[112:115], v[44:47], a[48:51]\n"
    "s_add_u32 m0, %[mb], 1088\n"
    "v_mfma_f32_16x16x32_bf16 a[52:55], v[116:119], v[44:47], a[52:55]\n"
    "buffer_load_dwordx4 %[vW], s[64:67], s70 offen sc1 lds\n"
    "v_mfma_f32_16x16x32_bf16 a[56:59], v[120:123], v[44:47], a[56:59]\n"
    "buffer_load_dwordx4 v[100:103], %[vW], s[60:63], s68 offen offset:1024 sc1\n"
    "v_mfma_f32_16x16x32_bf16 a[60:63], v[124:127], v[44:47], a[60:63]\n"
    "ds_read_b128 v[44:47], %[vA1] offset:11520\n"
    "s_waitcnt lgkmcnt(6)\n"
    "v_mfma_f32_16x16x32_bf16 a[64:67], v[112:115], v[48:51], a[64:67]\n"
    "v_mfma_f32_16x16x32_bf16 a[68:71], v[116:119], v[48:51], a[68:71]\n"
    "v_mfma_f32_16x16x32_bf16 a[72:75], v[120:123], v[48:51], a[72:75]\n"
    "v_mfma_f32_16x16x32_bf16 a[76:79], v[124:127], v[48:51], a[76:79]\n"
    "ds_read_b128 v[48:51], %[vA1] offset:12672\n"
    "v_mfma_f32_16x16x32_bf16 a[80:83], v[112:115], v[52:55], a[80:83]\n"
    "s_add_u32 s70, s69, 2048\n"
    "v_mfma_f32_16x16x32_bf16 a[84:87], v[116:119], v[52:55], a[84:87]\n"
    "s_add_u32 m0, %[mb], 2112\n"
    "v_mfma_f32_16x16x32_bf16 a[88:91], v[120:123], v[52:55], a[88:91]\n"
    "buffer_load_dwordx4 %[vW], s[64:67], s70 offen sc1 lds\n"
    "v_mfma_f32_16x16x32_bf16 a[92:95], v[124:127], v[52:55], a[92:95]\n"
    "ds_read_b128 v[52:55], %[vA1] offset:13696\n"
    "s_waitcnt lgkmcnt(6)\n"
    "v_mfma_f32_16x16x32_bf16 a[96:99], v[112:115], v[56:59], a[96:99]\n"
    "buffer_load_dwordx4 v[104:107], %[vW], s[60:63], s68 offen offset:2048 sc1\n"
    "v_mfma_f32_16x16x32_bf16 a[100:103], v[116:119], v[56:59], a[100:103]\n"
    "v_mfma_f32_16x16x32_bf16 a[104:107], v[120:123], v[56:59], a[104:107]\n"
    "buffer_load_dwordx4 v[108:111], %[vW], s[60:63], s68 offen offset:3072 sc1\n"
    "v_mfma_f32_16x16x32_bf16 a[108:111], v[124:127], v[56:59], a[108:111]\n"
    "ds_read_b128 v[56:59], %[vA1] offset:14720\n"
    "v_mfma_f32_16x16x32_bf16 a[112:115], v[112:115], v[60:63], a[112:115]\n"
    "s_sub_u32 s74, s74, 1\n"
    "s_cselect_b32 s73, s77, s76\n"
    "v_mfma_f32_16x16x32_bf16 a[116:119], v[116:119], v[60:63], a[116:119]\n"
    "s_add_u32 s68, s68, s73\n"
    "v_mfma_f32_16x16x32_bf16 a[120:123], v[120:123], v[60:63], a[120:123]\n"
    "v_mfma_f32_16x16x32_bf16 a[124:127], v[124:127], v[60:63], a[124:127]\n"
    "ds_read_b128 v[60:63], %[vA1] offset:15744\n"
    "s_waitcnt vmcnt(13) lgkmcnt(6)\n"
    "v_mfma_f32_16x16x32_bf16 a[0:3], v[64:67], v[32:35], a[0:3]\n"
    "s_add_u32 s70, s69, 3072\n"
    "v_mfma_f32_16x16x32_bf16 a[4:7], v[68:71], v[32:35], a[4:7]\n"
    "s_add_u32 m0, %[mb], 3136\n"
    "v_mfma_f32_16x16x32_bf16 a[8:11], v[72:75], v[32:35], a[8:11]\n"
    "buffer_load_dwordx4 %[vW], s[64:67], s70 offen sc1 lds\n"
    "v_mfma_f32_16x16x32_bf16 a[12:15], v[76:79], v[32:35], a[12:15]\n"
    "ds_read_b128 v[32:35], %[vA2] offset:8448\n"
    "v_mfma_f32_16x16x32_bf16 a[16:19], v[64:67], v[36:39], a[16:19]\n"
    "v_mfma_f32_16x16x32_bf16 a[20:23], v[68:71], v[36:39], a[20:23]\n"
    "buffer_load_dwordx4 v[112:115], %[vW], s[60:63], s68 offen offset:0 sc1\n"
    "v_mfma_f32_16x16x32_bf16 a[24:27], v[72:75], v[36:39], a[24:27]\n"
    "v_mfma_f32_16x16x32_bf16 a[28:31], v[76:79], v[36:39], a[28:31]\n"
    "ds_read_b128 v[36:39], %[vA2] offset:9472\n"
    "s_waitcnt lgkmcnt(6)\n"
    "v_mfma_f32_16x16x32_bf16 a[32:35], v[64:67], v[40:43], a[32:35]\n"
    "v_mfma_f32_16x16x32_bf16 a[36:39], v[68:71], v[40:43], a[36:39]\n"
    "v_mfma_f32_16x16x32_bf16 a[40:43], v[72:75], v[40:43], a[40:43]\n"
    "s_add_u32 s70, s69, 262144\n"
    "v_mfma_f32_16x16x32_bf16 a[44:47], v[76:79], v[40:43], a[44:47]\n"
    "ds_read_b128 v[40:43], %[vA2] offset:10496\n"
    "v_mfma_f32_16x16x32_bf16 a[48:51], v[64:67], v[44:47], a[48:51]\n"
    "s_add_u32 m0, %[mb], 4288\n"
    "v_mfma_f32_16x16x32_bf16 a[52:55], v[68:71], v[44:47], a[52:55]\n"
    "buffer_load_dwordx4 %[vW], s[64:67], s70 offen sc1 lds\n"
    "v_mfma_f32_16x16x32_bf16 a[56:59], v[72:75], v[44:47], a[56:59]\n"
    "buffer_load_dwordx4 v[116:119], %[vW], s[60:63], s68 offen offset:1024 sc1\n"
    "v_mfma_f32_16x16x32_bf16 a[60:63], v[76:79], v[44:47], a[60:63]\n"
    "ds_read_b128 v[44:47], %[vA2] offset:11520\n"
    "s_waitcnt lgkmcnt(6)\n"
    "v_mfma_f32_16x16x32_bf16 a[64:67], v[64:67], v[48:51], a[64:67]\n"
    "v_mfma_f32_16x16x32_bf16 a[68:71], v[68:71], v[48:51], a[68:71]\n"
    "v_mfma_f32_16x16x32_bf16 a[72:75], v[72:75], v[48:51], a[72:75]\n"
    "v_mfma_f32_16x16x32_bf16 a[76:79], v[76:79], v[48:51], a[76:79]\n"
    "ds_read_b128 v[48:51], %[vA2] offset:12672\n"
    "v_mfma_f32_16x16x32_bf16 a[80:83], v[64:67], v[52:55], a[80:83]\n"
    "s_add_u32 s70, s69, 263168\n"
    "v_mfma_f32_16x16x32_bf16 a[84:87], v[68:71], v[52:55], a[84:87]\n"
    "s_add_u32 m0, %[mb], 5312\n"
    "v_mfma_f32_16x16x32_bf16 a[88:91], v[72:75], v[52:55], a[88:91]\n"
    "buffer_load_dwordx4 %[vW], s[64:67], s70 offen sc1 lds\n"
    "v_mfma_f32_16x16x32_bf16 a[92:95], v[76:79], v[52:55], a[92:95]\n"
    "ds_read_b128 v[52:55], %[vA2] offset:13696\n"
    "s_waitcnt lgkmcnt(6)\n"
    "v_mfma_f32_16x16x32_bf16 a[96:99], v[64:67], v[56:59], a[96:99]\n"
    "buffer_load_dwordx4 v[120:123], %[vW], s[60:63], s68 offen offset:2048 sc1\n"
    "v_mfma_f32_16x16x32_bf16 a[100:103], v[68:71], v[56:59], a[100:103]\n"
    "v_mfma_f32_16x16x32_bf16 a[104:107], v[72:75], v[56:59], a[104:107]\n"
    "buffer_load_dwordx4 v[124:127], %[vW], s[60:63], s68 offen offset:3072 sc1\n"
    "v_mfma_f32_16x16x32_bf16 a[108:111], v[76:79], v[56:59], a[108:111]\n"
    "ds_read_b128 v[56:59], %[vA2] offset:14720\n"
    "v_mfma_f32_16x16x32_bf16 a[112:115], v[64:67], v[60:63], a[112:115]\n"
    "s_sub_u32 s74, s74, 1\n"
    "s_cselect_b32 s73, s77, s76\n"
    "v_mfma_f32_16x16x32_bf16 a[116:119], v[68:71], v[60:63], a[116:119]\n"
    "s_add_u32 s68, s68, s73\n"
    "v_mfma_f32_16x16x32_bf16 a[120:123], v[72:75], v[60:63], a[120:123]\n"
    "v_mfma_f32_16x16x32_bf16 a[124:127], v[76:79], v[60:63], a[124:127]\n"
    "ds_read_b128 v[60:63], %[vA2] offset:15744\n"
    "s_waitcnt vmcnt(14) lgkmcnt(6)\n"
    "v_mfma_f32_16x16x32_bf16 a[0:3], v[80:83], v[32:35], a[0:3]\n"
    "s_add_u32 s70, s69, 264192\n"
    "v_mfma_f32_16x16x32_bf16 a[4:7], v[84:87], v[32:35], a[4:7]\n"
    "s_add_u32 m0, %[mb], 6336\n"
    "v_mfma_f32_16x16x32_bf16 a[8:11], v[88:91], v[32:35], a[8:11]\n"
    "buffer_load_dwordx4 %[vW], s[64:67], s70 offen sc1 lds\n"
    "v_mfma_f32_16x16x32_bf16 a[12:15], v[92:95], v[32:35], a[12:15]\n"
    "ds_read_b128 v[32:35], %[vA0] offset:16896\n"
    "v_mfma_f32_16x16x32_bf16 a[16:19], v[80:83], v[36:39], a[16:19]\n"
    "v_mfma_f32_16x16x32_bf16 a[20:23], v[84:87], v[36:39], a[20:23]\n"
    "buffer_load_dwordx4 v[64:67], %[vW], s[60:63], s68 offen offset:0 sc1\n"
    "v_mfma_f32_16x16x32_bf16 a[24:27], v[88:91], v[36:39], a[24:27]\n"
    "v_mfma_f32_16x16x32_bf16 a[28:31], v[92:95], v[36:39], a[28:31]\n"
    "ds_read_b128 v[36:39], %[vA0] offset:17920\n"
    "s_waitcnt lgkmcnt(6)\n"
    "v_mfma_f32_16x16x32_bf16 a[32:35], v[80:83], v[40:43], a[32:35]\n"
    "v_mfma_f32_16x16x32_bf16 a[36:39], v[84:87], v[40:43], a[36:39]\n"
    "v_mfma_f32_16x16x32_bf16 a[40:43], v[88:91], v[40:43], a[40:43]\n"
    "v_mfma_f32_16x16x32_bf16 a[44:47], v[92:95], v[40:43], a[44:47]\n"
    "ds_read_b128 v[40:43], %[vA0] offset:18944\n"
    "v_mfma_f32_16x16x32_bf16 a[48:51], v[80:83], v[44:47], a[48:51]\n"
    "v_mfma_f32_16x16x32_bf16 a[52:55], v[84:87], v[44:47], a[52:55]\n"
    "buffer_load_dwordx4 v[68:71], %[vW], s[60:63], s68 offen offset:1024 sc1\n"
    "v_mfma_f32_16x16x32_bf16 a[56:59], v[88:91], v[44:47], a[56:59]\n"
    "v_mfma_f32_16x16x32_bf16 a[60:63], v[92:95], v[44:47], a[60:63]\n"
    "ds_read_b128 v[44:47], %[vA0] offset:19968\n"
    "s_waitcnt lgkmcnt(6)\n"
    "v_mfma_f32_16x16x32_bf16 a[64:67], v[80:83], v[48:51], a[64:67]\n"
    "s_add_u32 s70, s69, 265216\n"
    "v_mfma_f32_16x16x32_bf16 a[68:71], v[84:87], v[48:51], a[68:71]\n"
    "s_add_u32 m0, %[mb], 7360\n"
    "v_mfma_f32_16x16x32_bf16 a[72:75], v[88:91], v[48:51], a[72:75]\n"
    "buffer_load_dwordx4 %[vW], s[64:67], s70 offen sc1 lds\n"
    "v_mfma_f32_16x16x32_bf16 a[76:79], v[92:95], v[48:51], a[76:79]\n"
    "ds_read_b128 v[48:51], %[vA0] offset:21120\n"
    "v_mfma_f32_16x16x32_bf16 a[80:83], v[80:83], v[52:55], a[80:83]\n"
    "s_sub_u32 s75, s75, 1\n"
    "s_cselect_b32 s79, s78, s76\n"
    "v_mfma_f32_16x16x32_bf16 a[84:87], v[84:87], v[52:55], a[84:87]\n"
    "buffer_load_dwordx4 v[72:75], %[vW], s[60:63], s68 offen offset:2048 sc1\n"
    "v_mfma_f32_16x16x32_bf16 a[88:91], v[88:91], v[52:55], a[88:91]\n"
    "s_add_u32 s69, s69, s79\n"
    "v_mfma_f32_16x16x32_bf16 a[92:95], v[92:95], v[52:55], a[92:95]\n"
    "ds_read_b128 v[52:55], %[vA0] offset:22144\n"
    "s_waitcnt lgkmcnt(6)\n"
    "v_mfma_f32_16x16x32_bf16 a[96:99], v[80:83], v[56:59], a[96:99]\n"
    "v_mfma_f32_16x16x32_bf16 a[100:103], v[84:87], v[56:59], a[100:103]\n"
    "v_mfma_f32_16x16x32_bf16 a[104:107], v[88:91], v[56:59], a[104:107]\n"
    "buffer_load_dwordx4 v[76:79], %[vW], s[60:63], s68 offen offset:3072 sc1\n"
    "v_mfma_f32_16x16x32_bf16 a[108:111], v[92:95], v[56:59], a[108:111]\n"
    "ds_read_b128 v[56:59], %[vA0] offset:23168\n"
    "v_mfma_f32_16x16x32_bf16 a[112:115], v[80:83], v[60:63], a[112:115]\n"
    "s_sub_u32 s74, s74, 1\n"
    "s_cselect_b32 s73, s77, s76\n"
    "v_mfma_f32_16x16x32_bf16 a[116:119], v[84:87], v[60:63], a[116:119]\n"
    "s_add_u32 s68, s68, s73\n"
    "v_mfma_f32_16x16x32_bf16 a[120:123], v[88:91], v[60:63], a[120:123]\n"
    "v_mfma_f32_16x16x32_bf16 a[124:127], v[92:95], v[60:63], a[124:127]\n"
    "ds_read_b128 v[60:63], %[vA0] offset:24192\n"
    "s_add_u32 s71, s71, 1\n"
    "s_cmp_lt_u32 s71, 3\n"
    "s_cbranch_scc1 LOOP%=\n"
    "s_waitcnt vmcnt(13) lgkmcnt(6)\n"
    "v_mfma_f32_16x16x32_bf16 a[0:3], v[96:99], v[32:35], a[0:3]\n"
    "v_mfma_f32_16x16x32_bf16 a[4:7], v[100:103], v[32:35], a[4:7]\n"
    "v_mfma_f32_16x16x32_bf16 a[8:11], v[104:107], v[32:35], a[8:11]\n"
    "v_mfma_f32_16x16x32_bf16 a[12:15], v[108:111], v[32:35], a[12:15]\n"
    "ds_read_b128 v[32:35], %[vA1] offset:16896\n"
    "v_mfma_f32_16x16x32_bf16 a[16:19], v[96:99], v[36:39], a[16:19]\n"
    "v_mfma_f32_16x16x32_bf16 a[20:23], v[100:103], v[36:39], a[20:23]\n"
    "buffer_load_dwordx4 v[80:83], %[vW], s[60:63], s68 offen offset:0 sc1\n"
    "v_mfma_f32_16x16x32_bf16 a[24:27], v[104:107], v[36:39], a[24:27]\n"
    "v_mfma_f32_16x16x32_bf16 a[28:31], v[108:111], v[36:39], a[28:31]\n"
    "ds_read_b128 v[36:39], %[vA1] offset:17920\n"
    "s_waitcnt lgkmcnt(6)\n"
    "v_mfma_f32_16x16x32_bf16 a[32:35], v[96:99], v[40:43], a[32:35]\n"
    "v_mfma_f32_16x16x32_bf16 a[36:39], v[100:103], v[40:43], a[36:39]\n"
    "v_mfma_f32_16x16x32_bf16 a[40:43], v[104:107], v[40:43], a[40:43]\n"
    "v_mfma_f32_16x16x32_bf16 a[44:47], v[108:111], v[40:43], a[44:47]\n"
    "ds_read_b128 v[40:43], %[vA1] offset:18944\n"
    "v_mfma_f32_16x16x32_bf16 a[48:51], v[96:99], v[44:47], a[48:51]\n"
    "v_mfma_f32_16x16x32_bf16 a[52:55], v[100:103], v[44:47], a[52:55]\n"
    "buffer_load_dwordx4 v[84:87], %[vW], s[60:63], s68 offen offset:1024 sc1\n"
    "v_mfma_f32_16x16x32_bf16 a[56:59], v[104:107], v[44:47], a[56:59]\n"
    "v_mfma_f32_16x16x32_bf16 a[60:63], v[108:111], v[44:47], a[60:63]\n"
    "ds_read_b128 v[44:47], %[vA1] offset:19968\n"
    "s_waitcnt lgkmcnt(6)\n"
    "v_mfma_f32_16x16x32_bf16 a[64:67], v[96:99], v[48:51], a[64:67]\n"
    "v_mfma_f32_16x16x32_bf16 a[68:71], v[100:103], v[48:51], a[68:71]\n"
    "v_mfma_f32_16x16x32_bf16 a[72:75], v[104:107], v[48:51], a[72:75]\n"
    "v_mfma_f32_16x16x32_bf16 a[76:79], v[108:111], v[48:51], a[76:79]\n"
    "ds_read_b128 v[48:51], %[vA1] offset:21120\n"
    "v_mfma_f32_16x16x32_bf16 a[80:83], v[96:99], v[52:55], a[80:83]\n"
    "v_mfma_f32_16x16x32_bf16 a[84:87], v[100:103], v[52:55], a[84:87]\n"
    "buffer_load_dwordx4 v[88:91], %[vW], s[60:63], s68 offen offset:2048 sc1\n"
    "v_mfma_f32_16x16x32_bf16 a[88:91], v[104:107], v[52:55], a[88:91]\n"
    "v_mfma_f32_16x16x32_bf16 a[92:95], v[108:111], v[52:55], a[92:95]\n"
    "ds_read_b128 v[52:55], %[vA1] offset:22144\n"
    "s_waitcnt lgkmcnt(6)\n"
    "v_mfma_f32_16x16x32_bf16 a[96:99], v[96:99], v[56:59], a[96:99]\n"
    "v_mfma_f32_16x16x32_bf16 a[100:103], v[100:103], v[56:59], a[100:103]\n"
    "v_mfma_f32_16x16x32_bf16 a[104:107], v[104:107], v[56:59], a[104:107]\n"
    "buffer_load_dwordx4 v[92:95], %[vW], s[60:63], s68 offen offset:3072 sc1\n"
    "v_mfma_f32_16x16x32_bf16 a[108:111], v[108:111], v[56:59], a[108:111]\n"
    "ds_read_b128 v[56:59], %[vA1] offset:23168\n"
    "v_mfma_f32_16x16x32_bf16 a[112:115], v[96:99], v[60:63], a[112:115]\n"
    "s_sub_u32 s74, s74, 1\n"
    "s_cselect_b32 s73, s77, s76\n"
    "v_mfma_f32_16x16x32_bf16 a[116:119], v[100:103], v[60:63], a[116:119]\n"
    "s_add_u32 s68, s68, s73\n"
    "v_mfma_f32_16x16x32_bf16 a[120:123], v[104:107], v[60:63], a[120:123]\n"
    "v_mfma_f32_16x16x32_bf16 a[124:127], v[108:111], v[60:63], a[124:127]\n"
    "ds_read_b128 v[60:63], %[vA1] offset:24192\n"
    "s_waitcnt vmcnt(10) lgkmcnt(6)\n"
    "v_mfma_f32_16x16x32_bf16 a[0:3], v[112:115], v[32:35], a[0:3]\n"
    "v_mfma_f32_16x16x32_bf16 a[4:7], v[116:119], v[32:35], a[4:7]\n"
    "v_mfma_f32_16x16x32_bf16 a[8:11], v[120:123], v[32:35], a[8:11]\n"
    "v_mfma_f32_16x16x32_bf16 a[12:15], v[124:127], v[32:35], a[12:15]\n"
    "ds_read_b128 v[32:35], %[vA2] offset:16896\n"
    "v_mfma_f32_16x16x32_bf16 a[16:19], v[112:115], v[36:39], a[16:19]\n"
    "v_mfma_f32_16x16x32_bf16 a[20:23], v[116:119], v[36:39], a[20:23]\n"
    "buffer_load_dwordx4 v[96:99], %[vW], s[60:63], s68 offen offset:0 sc1\n"
    "v_mfma_f32_16x16x32_bf16 a[24:27], v[120:123], v[36:39], a[24:27]\n"
    "v_mfma_f32_16x16x32_bf16 a[28:31], v[124:127], v[36:39], a[28:31]\n"
    "ds_read_b128 v[36:39], %[vA2] offset:17920\n"
    "s_waitcnt lgkmcnt(6)\n"
    "v_mfma_f32_16x16x32_bf16 a[32:35], v[112:115], v[40:43], a[32:35]\n"
    "v_mfma_f32_16x16x32_bf16 a[36:39], v[116:119], v[40:43], a[36:39]\n"
    "v_mfma_f32_16x16x32_bf16 a[40:43], v[120:123], v[40:43], a[40:43]\n"
    "v_mfma_f32_16x16x32_bf16 a[44:47], v[124:127], v[40:43], a[44:47]\n"
    "ds_read_b128 v[40:43], %[vA2] offset:18944\n"
    "v_mfma_f32_16x16x32_bf16 a[48:51], v[112:115], v[44:47], a[48:51]\n"
    "v_mfma_f32_16x16x32_bf16 a[52:55], v[116:119], v[44:47], a[52:55]\n"
    "buffer_load_dwordx4 v[100:103], %[vW], s[60:63], s68 offen offset:1024 sc1\n"
    "v_mfma_f32_16x16x32_bf16 a[56:59], v[120:123], v[44:47], a[56:59]\n"
    "v_mfma_f32_16x16x32_bf16 a[60:63], v[124:127], v[44:47], a[60:63]\n"
    "ds_read_b128 v[44:47], %[vA2] offset:19968\n"
    "s_waitcnt lgkmcnt(6)\n"
    "v_mfma_f32_16x16x32_bf16 a[64:67], v[112:115], v[48:51], a[64:67]\n"
    "v_mfma_f32_16x16x32_bf16 a[68:71], v[116:119], v[48:51], a[68:71]\n"
    "v_mfma_f32_16x16x32_bf16 a[72:75], v[120:123], v[48:51], a[72:75]\n"
    "v_mfma_f32_16x16x32_bf16 a[76:79], v[124:127], v[48:51], a[76:79]\n"
    "ds_read_b128 v[48:51], %[vA2] offset:21120\n"
    "v_mfma_f32_16x16x32_bf16 a[80:83], v[112:115], v[52:55], a[80:83]\n"
    "v_mfma_f32_16x16x32_bf16 a[84:87], v[116:119], v[52:55], a[84:87]\n"
    "buffer_load_dwordx4 v[104:107], %[vW], s[60:63], s68 offen offset:2048 sc1\n"
    "v_mfma_f32_16x16x32_bf16 a[88:91], v[120:123], v[52:55], a[88:91]\n"
    "v_mfma_f32_16x16x32_bf16 a[92:95], v[124:127], v[52:55], a[92:95]\n"
    "ds_read_b128 v[52:55], %[vA2] offset:22144\n"
    "s_waitcnt lgkmcnt(6)\n"
    "v_mfma_f32_16x16x32_bf16 a[96:99], v[112:115], v[56:59], a[96:99]\n"
    "v_mfma_f32_16x16x32_bf16 a[100:103], v[116:119], v[56:59], a[100:103]\n"
    "v_mfma_f32_16x16x32_bf16 a[104:107], v[120:123], v[56:59], a[104:107]\n"
    "buffer_load_dwordx4 v[108:111], %[vW], s[60:63], s68 offen offset:3072 sc1\n"
    "v_mfma_f32_16x16x32_bf16 a[108:111], v[124:127], v[56:59], a[108:111]\n"
    "ds_read_b128 v[56:59], %[vA2] offset:23168\n"
    "v_mfma_f32_16x16x32_bf16 a[112:115], v[112:115], v[60:63], a[112:115]\n"
    "s_sub_u32 s74, s74, 1\n"
    "s_cselect_b32 s73, s77, s76\n"
    "v_mfma_f32_16x16x32_bf16 a[116:119], v[116:119], v[60:63], a[116:119]\n"
    "s_add_u32 s68, s68, s73\n"
    "v_mfma_f32_16x16x32_bf16 a[120:123], v[120:123], v[60:63], a[120:123]\n"
    "v_mov_b32 v31, 1\n"
    "v_mfma_f32_16x16x32_bf16 a[124:127], v[124:127], v[60:63], a[124:127]\n"
    "ds_read_b128 v[60:63], %[vA2] offset:24192\n"
    "ds_write_b32 %[vCnt], v31 offset:8\n"
    "s_waitcnt vmcnt(8) lgkmcnt(7)\n"
    "v_mfma_f32_16x16x32_bf16 a[0:3], v[64:67], v[32:35], a[0:3]\n"
    "v_mfma_f32_16x16x32_bf16 a[4:7], v[68:71], v[32:35], a[4:7]\n"
    "v_mfma_f32_16x16x32_bf16 a[8:11], v[72:75], v[32:35], a[8:11]\n"
    "v_mfma_f32_16x16x32_bf16 a[12:15], v[76:79], v[32:35], a[12:15]\n"
    "v_mfma_f32_16x16x32_bf16 a[16:19], v[64:67], v[36:39], a[16:19]\n"
    "v_mfma_f32_16x16x32_bf16 a[20:23], v[68:71], v[36:39], a[20:23]\n"
    "buffer_load_dwordx4 v[112:115], %[vW], s[60:63], s68 offen offset:0 sc1\n"
    "v_mfma_f32_16x16x32_bf16 a[24:27], v[72:75], v[36:39], a[24:27]\n"
    "v_mfma_f32_16x16x32_bf16 a[28:31], v[76:79], v[36:39], a[28:31]\n"
    "s_waitcnt lgkmcnt(5)\n"
    "v_mfma_f32_16x16x32_bf16 a[32:35], v[64:67], v[40:43], a[32:35]\n"
    "v_mfma_f32_16x16x32_bf16 a[36:39], v[68:71], v[40:43], a[36:39]\n"
    "v_mfma_f32_16x16x32_bf16 a[40:43], v[72:75], v[40:43], a[40:43]\n"
    "v_mfma_f32_16x16x32_bf16 a[44:47], v[76:79], v[40:43], a[44:47]\n"
    "v_mfma_f32_16x16x32_bf16 a[48:51], v[64:67], v[44:47], a[48:51]\n"
    "v_mfma_f32_16x16x32_bf16 a[52:55], v[68:71], v[44:47], a[52:55]\n"
    "buffer_load_dwordx4 v[116:119], %[vW], s[60:63], s68 offen offset:1024 sc1\n"
    "v_mfma_f32_16x16x32_bf16 a[56:59], v[72:75], v[44:47], a[56:59]\n"
    "v_mfma_f32_16x16x32_bf16 a[60:63], v[76:79], v[44:47], a[60:63]\n"
    "s_waitcnt lgkmcnt(3)\n"
    "v_mfma_f32_16x16x32_bf16 a[64:67], v[64:67], v[48:51], a[64:67]\n"
    "v_mfma_f32_16x16x32_bf16 a[68:71], v[68:71], v[48:51], a[68:71]\n"
    "v_mfma_f32_16x16x32_bf16 a[72:75], v[72:75], v[48:51], a[72:75]\n"
    "v_mfma_f32_16x16x32_bf16 a[76:79], v[76:79], v[48:51], a[76:79]\n"
    "v_mfma_f32_16x16x32_bf16 a[80:83], v[64:67], v[52:55], a[80:83]\n"
    "v_mfma_f32_16x16x32_bf16 a[84:87], v[68:71], v[52:55], a[84:87]\n"
    "buffer_load_dwordx4 v[120:123], %[vW], s[60:63], s68 offen offset:2048 sc1\n"
    "v_mfma_f32_16x16x32_bf16 a[88:91], v[72:75], v[52:55], a[88:91]\n"
    "v_mfma_f32_16x16x32_bf16 a[92:95], v[76:79], v[52:55], a[92:95]\n"
    "s_waitcnt lgkmcnt(1)\n"
    "v_mfma_f32_16x16x32_bf16 a[96:99], v[64:67], v[56:59], a[96:99]\n"
    "v_mfma_f32_16x16x32_bf16 a[100:103], v[68:71], v[56:59], a[100:103]\n"
    "v_mfma_f32_16x16x32_bf16 a[104:107], v[72:75], v[56:59], a[104:107]\n"
    "buffer_load_dwordx4 v[124:127], %[vW], s[60:63], s68 offen offset:3072 sc1\n"
    "v_mfma_f32_16x16x32_bf16 a[108:111], v[76:79], v[56:59], a[108:111]\n"
    "s_sub_u32 s74, s74, 1\n"
    "s_cselect_b32 s73, s77, s76\n"
    "v_mfma_f32_16x16x32_bf16 a[112:115], v[64:67], v[60:63], a[112:115]\n"
    "s_add_u32 s68, s68, s73\n"
    "v_mfma_f32_16x16x32_bf16 a[116:119], v[68:71], v[60:63], a[116:119]\n"
    "v_mfma_f32_16x16x32_bf16 a[120:123], v[72:75], v[60:63], a[120:123]\n"
    "v_mfma_f32_16x16x32_bf16 a[124:127], v[76:79], v[60:63], a[124:127]\n"
    "s_waitcnt vmcnt(34)\n"
    "ds_read_b128 v[32:35], %[vA0] offset:25344\n"
    "ds_read_b128 v[36:39], %[vA1] offset:25344\n"
    "ds_read_b128 v[40:43], %[vA2] offset:25344\n"
    "ds_read_b128 v[44:47], %[vA0] offset:26368\n"
    "ds_read_b128 v[48:51], %[vA1] offset:26368\n"
    "ds_read_b128 v[52:55], %[vA2] offset:26368\n"
    "ds_read_b128 v[56:59], %[vA0] offset:27392\n"
    "ds_read_b128 v[60:63], %[vA1] offset:27392\n"
    "s_waitcnt vmcnt(8) lgkmcnt(7)\n"
    "v_mfma_f32_16x16x32_bf16 a[0:3], v[80:83], v[32:35], a[0:3]\n"
    "v_mfma_f32_16x16x32_bf16 a[4:7], v[84:87], v[32:35], a[4:7]\n"
    "v_mfma_f32_16x16x32_bf16 a[8:11], v[88:91], v[32:35], a[8:11]\n"
    "v_mfma_f32_16x16x32_bf16 a[12:15], v[92:95], v[32:35], a[12:15]\n"
    "ds_read_b128 v[32:35], %[vA2] offset:27392\n"
    "s_waitcnt vmcnt(4) lgkmcnt(7)\n"
    "v_mfma_f32_16x16x32_bf16 a[0:3], v[96:99], v[36:39], a[0:3]\n"
    "v_mfma_f32_16x16x32_bf16 a[4:7], v[100:103], v[36:39], a[4:7]\n"
    "v_mfma_f32_16x16x32_bf16 a[8:11], v[104:107], v[36:39], a[8:11]\n"
    "v_mfma_f32_16x16x32_bf16 a[12:15], v[108:111], v[36:39], a[12:15]\n"
    "ds_read_b128 v[36:39], %[vA0] offset:28416\n"
    "s_waitcnt vmcnt(0) lgkmcnt(7)\n"
    "v_mfma_f32_16x16x32_bf16 a[0:3], v[112:115], v[40:43], a[0:3]\n"
    "v_mfma_f32_16x16x32_bf16 a[4:7], v[116:119], v[40:43], a[4:7]\n"
    "v_mfma_f32_16x16x32_bf16 a[8:11], v[120:123], v[40:43], a[8:11]\n"
    "v_mfma_f32_16x16x32_bf16 a[12:15], v[124:127], v[40:43], a[12:15]\n"
    "ds_read_b128 v[40:43], %[vA1] offset:28416\n"
    "s_waitcnt lgkmcnt(7)\n"
    "v_mfma_f32_16x16x32_bf16 a[16:19], v[80:83], v[44:47], a[16:19]\n"
    "v_mfma_f32_16x16x32_bf16 a[20:23], v[84:87], v[44:47], a[20:23]\n"
    "v_mfma_f32_16x16x32_bf16 a[24:27], v[88:91], v[44:47], a[24:27]\n"
    "v_mfma_f32_16x16x32_bf16 a[28:31], v[92:95], v[44:47], a[28:31]\n"
    "ds_read_b128 v[44:47], %[vA2] offset:28416\n"
    "s_waitcnt lgkmcnt(7)\n"
    "v_mfma_f32_16x16x32_bf16 a[16:19], v[96:99], v[48:51], a[16:19]\n"
    "v_mfma_f32_16x16x32_bf16 a[20:23], v[100:103], v[48:51], a[20:23]\n"
    "v_mfma_f32_16x16x32_bf16 a[24:27], v[104:107], v[48:51], a[24:27]\n"
    "v_mfma_f32_16x16x32_bf16 a[28:31], v[108:111], v[48:51], a[28:31]\n"
    "ds_read_b128 v[48:51], %[vA0] offset:29568\n"
    "s_waitcnt lgkmcnt(7)\n"
    "v_mfma_f32_16x16x32_bf16 a[16:19], v[112:115], v[52:55], a[16:19]\n"
    "ds_write_b128 %[vE], a[0:3] offset:0\n"
    "v_mfma_f32_16x16x32_bf16 a[20:23], v[116:119], v[52:55], a[20:23]\n"
    "ds_write_b128 %[vE], a[4:7] offset:64\n"
    "v_mfma_f32_16x16x32_bf16 a[24:27], v[120:123], v[52:55], a[24:27]\n"
    "ds_write_b128 %[vE], a[8:11] offset:128\n"
    "v_mfma_f32_16x16x32_bf16 a[28:31], v[124:127], v[52:55], a[28:31]\n"
    "ds_read_b128 v[52:55], %[vA1] offset:29568\n"
    "s_waitcnt lgkmcnt(10)\n"
    "v_mfma_f32_16x16x32_bf16 a[32:35], v[80:83], v[56:59], a[32:35]\n"
    "ds_write_b128 %[vE], a[12:15] offset:192\n"
    "v_mfma_f32_16x16x32_bf16 a[36:39], v[84:87], v[56:59], a[36:39]\n"
    "v_mfma_f32_16x16x32_bf16 a[40:43], v[88:91], v[56:59], a[40:43]\n"
    "v_mfma_f32_16x16x32_bf16 a[44:47], v[92:95], v[56:59], a[44:47]\n"
    "ds_read_b128 v[56:59], %[vA2] offset:29568\n"
    "s_waitcnt lgkmcnt(11)\n"
    "v_mfma_f32_16x16x32_bf16 a[32:35], v[96:99], v[60:63], a[32:35]\n"
    "v_mfma_f32_16x16x32_bf16 a[36:39], v[100:103], v[60:63], a[36:39]\n"
    "v_mfma_f32_16x16x32_bf16 a[40:43], v[104:107], v[60:63], a[40:43]\n"
    "v_mfma_f32_16x16x32_bf16 a[44:47], v[108:111], v[60:63], a[44:47]\n"
    "ds_read_b128 v[60:63], %[vA0] offset:30592\n"
    "s_waitcnt lgkmcnt(11)\n"
    "v_mfma_f32_16x16x32_bf16 a[32:35], v[112:115], v[32:35], a[32:35]\n"
    "ds_write_b128 %[vE], a[16:19] offset:4352\n"
    "v_mfma_f32_16x16x32_bf16 a[36:39], v[116:119], v[32:35], a[36:39]\n"
    "ds_write_b128 %[vE], a[20:23] offset:4416\n"
    "v_mfma_f32_16x16x32_bf16 a[40:43], v[120:123], v[32:35], a[40:43]\n"
    "ds_write_b128 %[vE], a[24:27] offset:4480\n"
    "v_mfma_f32_16x16x32_bf16 a[44:47], v[124:127], v[32:35], a[44:47]\n"
    "ds_read_b128 v[32:35], %[vA1] offset:30592\n"
    "s_waitcnt lgkmcnt(14)\n"
    "v_mfma_f32_16x16x32_bf16 a[48:51], v[80:83], v[36:39], a[48:51]\n"
    "ds_write_b128 %[vE], a[28:31] offset:4544\n"
    "v_mfma_f32_16x16x32_bf16 a[52:55], v[84:87], v[36:39], a[52:55]\n"
    "v_mfma_f32_16x16x32_bf16 a[56:59], v[88:91], v[36:39], a[56:59]\n"
    "v_mfma_f32_16x16x32_bf16 a[60:63], v[92:95], v[36:39], a[60:63]\n"
    "ds_read_b128 v[36:39], %[vA2] offset:30592\n"
    "s_waitcnt lgkmcnt(15)\n"
    "v_mfma_f32_16x16x32_bf16 a[48:51], v[96:99], v[40:43], a[48:51]\n"
    "v_mfma_f32_16x16x32_bf16 a[52:55], v[100:103], v[40:43], a[52:55]\n"
    "v_mfma_f32_16x16x32_bf16 a[56:59], v[104:107], v[40:43], a[56:59]\n"
    "v_mfma_f32_16x16x32_bf16 a[60:63], v[108:111], v[40:43], a[60:63]\n"
    "ds_read_b128 v[40:43], %[vA0] offset:31616\n"
    "s_waitcnt lgkmcnt(15)\n"
    "v_mfma_f32_16x16x32_bf16 a[48:51], v[112:115], v[44:47], a[48:51]\n"
    "ds_write_b128 %[vE], a[32:35] offset:8704\n"
    "v_mfma_f32_16x16x32_bf16 a[52:55], v[116:119], v[44:47], a[52:55]\n"
    "ds_write_b128 %[vE], a[36:39] offset:8768\n"
    "v_mfma_f32_16x16x32_bf16 a[56:59], v[120:123], v[44:47], a[56:59]\n"
    "ds_write_b128 %[vE], a[40:43] offset:8832\n"
    "v_mfma_f32_16x16x32_bf16 a[60:63], v[124:127], v[44:47], a[60:63]\n"
    "ds_read_b128 v[44:47], %[vA1] offset:31616\n"
    "s_waitcnt lgkmcnt(15)\n"
    "v_mfma_f32_16x16x32_bf16 a[64:67], v[80:83], v[48:51], a[64:67]\n"
    "ds_write_b128 %[vE], a[44:47] offset:8896\n"
    "v_mfma_f32_16x16x32_bf16 a[68:71], v[84:87], v[48:51], a[68:71]\n"
    "v_mfma_f32_16x16x32_bf16 a[72:75], v[88:91], v[48:51], a[72:75]\n"
    "v_mfma_f32_16x16x32_bf16 a[76:79], v[92:95], v[48:51], a[76:79]\n"
    "ds_read_b128 v[48:51], %[vA2] offset:31616\n"
    "s_waitcnt lgkmcnt(15)\n"
    "v_mfma_f32_16x16x32_bf16 a[64:67], v[96:99], v[52:55], a[64:67]\n"
    "v_mfma_f32_16x16x32_bf16 a[68:71], v[100:103], v[52:55], a[68:71]\n"
    "v_mfma_f32_16x16x32_bf16 a[72:75], v[104:107], v[52:55], a[72:75]\n"
    "v_mfma_f32_16x16x32_bf16 a[76:79], v[108:111], v[52:55], a[76:79]\n"
    "ds_read_b128 v[52:55], %[vA0] offset:32640\n"
    "s_waitcnt lgkmcnt(15)\n"
    "v_mfma_f32_16x16x32_bf16 a[64:67], v[112:115], v[56:59], a[64:67]\n"
    "ds_write_b128 %[vE], a[48:51] offset:13056\n"
    "v_mfma_f32_16x16x32_bf16 a[68:71], v[116:119], v[56:59], a[68:71]\n"
    "ds_write_b128 %[vE], a[52:55] offset:13120\n"
    "v_mfma_f32_16x16x32_bf16 a[72:75], v[120:123], v[56:59], a[72:75]\n"
    "ds_write_b128 %[vE], a[56:59] offset:13184\n"
    "v_mfma_f32_16x16x32_bf16 a[76:79], v[124:127], v[56:59], a[76:79]\n"
    "ds_read_b128 v[56:59], %[vA1] offset:32640\n"
    "s_waitcnt lgkmcnt(15)\n"
    "v_mfma_f32_16x16x32_bf16 a[80:83], v[80:83], v[60:63], a[80:83]\n"
    "ds_write_b128 %[vE], a[60:63] offset:13248\n"
    "v_mfma_f32_16x16x32_bf16 a[84:87], v[84:87], v[60:63], a[84:87]\n"
    "v_mfma_f32_16x16x32_bf16 a[88:91], v[88:91], v[60:63], a[88:91]\n"
    "v_mfma_f32_16x16x32_bf16 a[92:95], v[92:95], v[60:63], a[92:95]\n"
    "ds_read_b128 v[60:63], %[vA2] offset:32640\n"
    "s_waitcnt lgkmcnt(15)\n"
    "v_mfma_f32_16x16x32_bf16 a[80:83], v[96:99], v[32:35], a[80:83]\n"
    "v_mfma_f32_16x16x32_bf16 a[84:87], v[100:103], v[32:35], a[84:87]\n"
    "v_mfma_f32_16x16x32_bf16 a[88:91], v[104:107], v[32:35], a[88:91]\n"
    "v_mfma_f32_16x16x32_bf16 a[92:95], v[108:111], v[32:35], a[92:95]\n"
    "s_waitcnt lgkmcnt(14)\n"
    "v_mfma_f32_16x16x32_bf16 a[80:83], v[112:115], v[36:39], a[80:83]\n"
    "ds_write_b128 %[vE], a[64:67] offset:17408\n"
    "v_mfma_f32_16x16x32_bf16 a[84:87], v[116:119], v[36:39], a[84:87]\n"
    "ds_write_b128 %[vE], a[68:71] offset:17472\n"
    "v_mfma_f32_16x16x32_bf16 a[88:91], v[120:123], v[36:39], a[88:91]\n"
    "ds_write_b128 %[vE], a[72:75] offset:17536\n"
    "v_mfma_f32_16x16x32_bf16 a[92:95], v[124:127], v[36:39], a[92:95]\n"
    "s_waitcnt lgkmcnt(15)\n"
    "v_mfma_f32_16x16x32_bf16 a[96:99], v[80:83], v[40:43], a[96:99]\n"
    "ds_write_b128 %[vE], a[76:79] offset:17600\n"
    "v_mfma_f32_16x16x32_bf16 a[100:103], v[84:87], v[40:43], a[100:103]\n"
    "v_mfma_f32_16x16x32_bf16 a[104:107], v[88:91], v[40:43], a[104:107]\n"
    "v_mfma_f32_16x16x32_bf16 a[108:111], v[92:95], v[40:43], a[108:111]\n"
    "s_waitcnt lgkmcnt(13)\n"
    "v_mfma_f32_16x16x32_bf16 a[96:99], v[96:99], v[44:47], a[96:99]\n"
    "v_mfma_f32_16x16x32_bf16 a[100:103], v[100:103], v[44:47], a[100:103]\n"
    "v_mfma_f32_16x16x32_bf16 a[104:107], v[104:107], v[44:47], a[104:107]\n"
    "v_mfma_f32_16x16x32_bf16 a[108:111], v[108:111], v[44:47], a[108:111]\n"
    "s_waitcnt lgkmcnt(11)\n"
    "v_mfma_f32_16x16x32_bf16 a[96:99], v[112:115], v[48:51], a[96:99]\n"
    "ds_write_b128 %[vE], a[80:83] offset:21760\n"
    "v_mfma_f32_16x16x32_bf16 a[100:103], v[116:119], v[48:51], a[100:103]\n"
    "ds_write_b128 %[vE], a[84:87] offset:21824\n"
    "v_mfma_f32_16x16x32_bf16 a[104:107], v[120:123], v[48:51], a[104:107]\n"
    "ds_write_b128 %[vE], a[88:91] offset:21888\n"
    "v_mfma_f32_16x16x32_bf16 a[108:111], v[124:127], v[48:51], a[108:111]\n"
    "s_waitcnt lgkmcnt(13)\n"
    "v_mfma_f32_16x16x32_bf16 a[112:115], v[80:83], v[52:55], a[112:115]\n"
    "ds_write_b128 %[vE], a[92:95] offset:21952\n"
    "v_mfma_f32_16x16x32_bf16 a[116:119], v[84:87], v[52:55], a[116:119]\n"
    "v_mfma_f32_16x16x32_bf16 a[120:123], v[88:91], v[52:55], a[120:123]\n"
    "v_mfma_f32_16x16x32_bf16 a[124:127], v[92:95], v[52:55], a[124:127]\n"
    "s_waitcnt lgkmcnt(10)\n"
    "v_mfma_f32_16x16x32_bf16 a[112:115], v[96:99], v[56:59], a[112:115]\n"
    "v_mfma_f32_16x16x32_bf16 a[116:119], v[100:103], v[56:59], a[116:119]\n"
    "v_mfma_f32_16x16x32_bf16 a[120:123], v[104:107], v[56:59], a[120:123]\n"
    "v_mfma_f32_16x16x32_bf16 a[124:127], v[108:111], v[56:59], a[124:127]\n"
    "s_waitcnt lgkmcnt(8)\n"
    "v_mfma_f32_16x16x32_bf16 a[112:115], v[112:115], v[60:63], a[112:115]\n"
    "ds_write_b128 %[vE], a[96:99] offset:26112\n"
    "v_mfma_f32_16x16x32_bf16 a[116:119], v[116:119], v[60:63], a[116:119]\n"
    "ds_write_b128 %[vE], a[100:103] offset:26176\n"
    "v_mfma_f32_16x16x32_bf16 a[120:123], v[120:123], v[60:63], a[120:123]\n"
    "ds_write_b128 %[vE], a[104:107] offset:26240\n"
    "v_mfma_f32_16x16x32_bf16 a[124:127], v[124:127], v[60:63], a[124:127]\n"
    "s_nop 7\n"
    "ds_write_b128 %[vE], a[108:111] offset:26304\n"
    "ds_write_b128 %[vE], a[112:115] offset:30464\n"
    "ds_write_b128 %[vE], a[116:119] offset:30528\n"
    "ds_write_b128 %[vE], a[120:123] offset:30592\n"
    "ds_write_b128 %[vE], a[124:127] offset:30656\n"
    "s_waitcnt vmcnt(0) lgkmcnt(0)\n"
        CONV_OPERANDS);
    } else {
        asm volatile(
    "s_mov_b32 s60, %[wlo]\n"
    "s_and_b32 s61, %[whi], 0xffff\n"
    "s_mov_b32 s62, 196608\n"
    "s_mov_b32 s63, 0x20000\n"
    "s_mov_b32 s68, %[uoff]\n"
    "buffer_load_dwordx4 v[64:67], %[vW], s[60:63], s68 offen offset:0 sc1\n"
    "buffer_load_dwordx4 v[68:71], %[vW], s[60:63], s68 offen offset:1024 sc1\n"
    "buffer_load_dwordx4 v[72:75], %[vW], s[60:63], s68 offen offset:2048 sc1\n"
    "buffer_load_dwordx4 v[76:79], %[vW], s[60:63], s68 offen offset:3072 sc1\n"
    "s_add_u32 s68, s68, 4096\n"
    "buffer_load_dwordx4 v[80:83], %[vW], s[60:63], s68 offen offset:0 sc1\n"
    "buffer_load_dwordx4 v[84:87], %[vW], s[60:63], s68 offen offset:1024 sc1\n"
    "buffer_load_dwordx4 v[88:91], %[vW], s[60:63], s68 offen offset:2048 sc1\n"
    "buffer_load_dwordx4 v[92:95], %[vW], s[60:63], s68 offen offset:3072 sc1\n"
    "s_add_u32 s68, s68, 4096\n"
    "buffer_load_dwordx4 v[96:99], %[vW], s[60:63], s68 offen offset:0 sc1\n"
    "buffer_load_dwordx4 v[100:103], %[vW], s[60:63], s68 offen offset:1024 sc1\n"
    "buffer_load_dwordx4 v[104:107], %[vW], s[60:63], s68 offen offset:2048 sc1\n"
    "buffer_load_dwordx4 v[108:111], %[vW], s[60:63], s68 offen offset:3072 sc1\n"
    "s_add_u32 s68, s68, 4096\n"
    "s_mov_b32 s64, %[alo]\n"
    "s_and_b32 s65, %[ahi], 0xffff\n"
    "s_mov_b32 s66, 327680\n"
    "s_mov_b32 s67, 0x20000\n"
    "s_mov_b32 s69, %[vst4]\n"
    "s_mov_b32 s71, 0\n"
    "s_mov_b32 s74, %[ucnt]\n"
    "s_mov_b32 s75, %[vcnt]\n"
    "s_mov_b32 s76, 4096\n"
    "s_mov_b32 s77, 28672\n"
    "s_mov_b32 s78, 12288\n"
    "s_waitcnt lgkmcnt(0)\n"
    "ds_read_b128 v[32:35], %[vA0] offset:0\n"
    "ds_read_b128 v[36:39], %[vA0] offset:1024\n"
    "ds_read_b128 v[40:43], %[vA0] offset:2048\n"
    "ds_read_b128 v[44:47], %[vA0] offset:3072\n"
    "ds_read_b128 v[48:51], %[vA0] offset:4224\n"
    "ds_read_b128 v[52:55], %[vA0] offset:5248\n"
    "ds_read_b128 v[56:59], %[vA0] offset:6272\n"
    "ds_read_b128 v[60:63], %[vA0] offset:7296\n"
    "s_waitcnt vmcnt(0)\n"
    "s_mov_b64 exec, 1\n"
    "v_mov_b32 v31, 1\n"
    "ds_add_rtn_u32 v31, %[vCnt], v31\n"
    "s_waitcnt lgkmcnt(0)\n"
    "v_cmp_eq_u32 vcc, 7, v31\n"
    "v_mov_b32 v31, %[tag]\n"
    "s_cbranch_vccz NOFLAG%=\n"
    "global_store_dword %[vFo], v31, %[fbase] sc1\n"
    "NOFLAG%=:\n"
    "s_mov_b64 exec, -1\n"
    "s_nop 4\n"
    "s_waitcnt vmcnt(8) lgkmcnt(6)\n"
    "v_mfma_f32_16x16x32_bf16 a[0:3], v[32:35], v[64:67], 0\n"
    "v_mfma_f32_16x16x32_bf16 a[4:7], v[32:35], v[68:71], 0\n"
    "v_mfma_f32_16x16x32_bf16 a[8:11], v[32:35], v[72:75], 0\n"
    "v_mfma_f32_16x16x32_bf16 a[12:15], v[32:35], v[76:79], 0\n"
    "ds_read_b128 v[32:35], %[vA1] offset:0\n"
    "v_mfma_f32_16x16x32_bf16 a[16:19], v[36:39], v[64:67], 0\n"
    "v_mfma_f32_16x16x32_bf16 a[20:23], v[36:39], v[68:71], 0\n"
    "buffer_load_dwordx4 v[112:115], %[vW], s[60:63], s68 offen offset:0 sc1\n"
    "v_mfma_f32_16x16x32_bf16 a[24:27], v[36:39], v[72:75], 0\n"
    "v_mfma_f32_16x16x32_bf16 a[28:31], v[36:39], v[76:79], 0\n"
    "ds_read_b128 v[36:39], %[vA1] offset:1024\n"
    "s_waitcnt lgkmcnt(6)\n"
    "v_mfma_f32_16x16x32_bf16 a[32:35], v[40:43], v[64:67], 0\n"
    "v_mfma_f32_16x16x32_bf16 a[36:39], v[40:43], v[68:71], 0\n"
    "v_mfma_f32_16x16x32_bf16 a[40:43], v[40:43], v[72:75], 0\n"
    "v_mfma_f32_16x16x32_bf16 a[44:47], v[40:43], v[76:79], 0\n"
    "ds_read_b128 v[40:43], %[vA1] offset:2048\n"
    "v_mfma_f32_16x16x32_bf16 a[48:51], v[44:47], v[64:67], 0\n"
    "v_mfma_f32_16x16x32_bf16 a[52:55], v[44:47], v[68:71], 0\n"
    "buffer_load_dwordx4 v[116:119], %[vW], s[60:63], s68 offen offset:1024 sc1\n"
    "v_mfma_f32_16x16x32_bf16 a[56:59], v[44:47], v[72:75], 0\n"
    "v_mfma_f32_16x16x32_bf16 a[60:63], v[44:47], v[76:79], 0\n"
    "ds_read_b128 v[44:47], %[vA1] offset:3072\n"
    "s_waitcnt lgkmcnt(6)\n"
    "v_mfma_f32_16x16x32_bf16 a[64:67], v[48:51], v[64:67], 0\n"
    "v_mfma_f32_16x16x32_bf16 a[68:71], v[48:51], v[68:71], 0\n"
    "v_mfma_f32_16x16x32_bf16 a[72:75], v[48:51], v[72:75], 0\n"
    "v_mfma_f32_16x16x32_bf16 a[76:79], v[48:51], v[76:79], 0\n"
    "ds_read_b128 v[48:51], %[vA1] offset:4224\n"
    "v_mfma_f32_16x16x32_bf16 a[80:83], v[52:55], v[64:67], 0\n"
    "v_mfma_f32_16x16x32_bf16 a[84:87], v[52:55], v[68:71], 0\n"
    "buffer_load_dwordx4 v[120:123], %[vW], s[60:63], s68 offen offset:2048 sc1\n"
    "v_mfma_f32_16x16x32_bf16 a[88:91], v[52:55], v[72:75], 0\n"
    "v_mfma_f32_16x16x32_bf16 a[92:95], v[52:55], v[76:79], 0\n"
    "ds_read_b128 v[52:55], %[vA1] offset:5248\n"
    "s_waitcnt lgkmcnt(6)\n"
    "v_mfma_f32_16x16x32_bf16 a[96:99], v[56:59], v[64:67], 0\n"
    "v_mfma_f32_16x16x32_bf16 a[100:103], v[56:59], v[68:71], 0\n"
    "v_mfma_f32_16x16x32_bf16 a[104:107], v[56:59], v[72:75], 0\n"
    "buffer_load_dwordx4 v[124:127], %[vW], s[60:63], s68 offen offset:3072 sc1\n"
    "v_mfma_f32_16x16x32_bf16 a[108:111], v[56:59], v[76:79], 0\n"
    "ds_read_b128 v[56:59], %[vA1] offset:6272\n"
    "v_mfma_f32_16x16x32_bf16 a[112:115], v[60:63], v[64:67], 0\n"
    "s_add_u32 s68, s68, 4096\n"
    "v_mfma_f32_16x16x32_bf16 a[116:119], v[60:63], v[68:71], 0\n"
    "v_mfma_f32_16x16x32_bf16 a[120:123], v[60:63], v[72:75], 0\n"
    "v_mfma_f32_16x16x32_bf16 a[124:127], v[60:63], v[76:79], 0\n"
    "ds_read_b128 v[60:63], %[vA1] offset:7296\n"
    "s_waitcnt vmcnt(8) lgkmcnt(6)\n"
    "v_mfma_f32_16x16x32_bf16 a[0:3], v[32:35], v[80:83], a[0:3]\n"
    "v_mfma_f32_16x16x32_bf16 a[4:7], v[32:35], v[84:87], a[4:7]\n"
    "v_mfma_f32_16x16x32_bf16 a[8:11], v[32:35], v[88:91], a[8:11]\n"
    "v_mfma_f32_16x16x32_bf16 a[12:15], v[32:35], v[92:95], a[12:15]\n"
    "ds_read_b128 v[32:35], %[vA2] offset:0\n"
    "v_mfma_f32_16x16x32_bf16 a[16:19], v[36:39], v[80:83], a[16:19]\n"
    "v_mfma_f32_16x16x32_bf16 a[20:23], v[36:39], v[84:87], a[20:23]\n"
    "buffer_load_dwordx4 v[64:67], %[vW], s[60:63], s68 offen offset:0 sc1\n"
    "v_mfma_f32_16x16x32_bf16 a[24:27], v[36:39], v[88:91], a[24:27]\n"
    "v_mfma_f32_16x16x32_bf16 a[28:31], v[36:39], v[92:95], a[28:31]\n"
    "ds_read_b128 v[36:39], %[vA2] offset:1024\n"
    "s_waitcnt lgkmcnt(6)\n"
    "v_mfma_f32_16x16x32_bf16 a[32:35], v[40:43], v[80:83], a[32:35]\n"
    "v_mfma_f32_16x16x32_bf16 a[36:39], v[40:43], v[84:87], a[36:39]\n"
    "v_mfma_f32_16x16x32_bf16 a[40:43], v[40:43], v[88:91], a[40:43]\n"
    "v_mfma_f32_16x16x32_bf16 a[44:47], v[40:43], v[92:95], a[44:47]\n"
    "ds_read_b128 v[40:43], %[vA2] offset:2048\n"
    "v_mfma_f32_16x16x32_bf16 a[48:51], v[44:47], v[80:83], a[48:51]\n"
    "v_mfma_f32_16x16x32_bf16 a[52:55], v[44:47], v[84:87], a[52:55]\n"
    "buffer_load_dwordx4 v[68:71], %[vW], s[60:63], s68 offen offset:1024 sc1\n"
    "v_mfma_f32_16x16x32_bf16 a[56:59], v[44:47], v[88:91], a[56:59]\n"
    "v_mfma_f32_16x16x32_bf16 a[60:63], v[44:47], v[92:95], a[60:63]\n"
    "ds_read_b128 v[44:47], %[vA2] offset:3072\n"
    "s_waitcnt lgkmcnt(6)\n"
    "v_mfma_f32_16x16x32_bf16 a[64:67], v[48:51], v[80:83], a[64:67]\n"
    "v_mfma_f32_16x16x32_bf16 a[68:71], v[48:51], v[84:87], a[68:71]\n"
    "v_mfma_f32_16x16x32_bf16 a[72:75], v[48:51], v[88:91], a[72:75]\n"
    "v_mfma_f32_16x16x32_bf16 a[76:79], v[48:51], v[92:95], a[76:79]\n"
    "ds_read_b128 v[48:51], %[vA2] offset:4224\n"
    "v_mfma_f32_16x16x32_bf16 a[80:83], v[52:55], v[80:83], a[80:83]\n"
    "v_mfma_f32_16x16x32_bf16 a[84:87], v[52:55], v[84:87], a[84:87]\n"
    "buffer_load_dwordx4 v[72:75], %[vW], s[60:63], s68 offen offset:2048 sc1\n"
    "v_mfma_f32_16x16x32_bf16 a[88:91], v[52:55], v[88:91], a[88:91]\n"
    "v_mfma_f32_16x16x32_bf16 a[92:95], v[52:55], v[92:95], a[92:95]\n"
    "ds_read_b128 v[52:55], %[vA2] offset:5248\n"
    "s_waitcnt lgkmcnt(6)\n"
    "v_mfma_f32_16x16x32_bf16 a[96:99], v[56:59], v[80:83], a[96:99]\n"
    "v_mfma_f32_16x16x32_bf16 a[100:103], v[56:59], v[84:87], a[100:103]\n"
    "v_mfma_f32_16x16x32_bf16 a[104:107], v[56:59], v[88:91], a[104:107]\n"
    "buffer_load_dwordx4 v[76:79], %[vW], s[60:63], s68 offen offset:3072 sc1\n"
    "v_mfma_f32_16x16x32_bf16 a[108:111], v[56:59], v[92:95], a[108:111]\n"
    "ds_read_b128 v[56:59], %[vA2] offset:6272\n"
    "v_mfma_f32_16x16x32_bf16 a[112:115], v[60:63], v[80:83], a[112:115]\n"
    "s_add_u32 s68, s68, 4096\n"
    "v_mfma_f32_16x16x32_bf16 a[116:119], v[60:63], v[84:87], a[116:119]\n"
    "v_mfma_f32_16x16x32_bf16 a[120:123], v[60:63], v[88:91], a[120:123]\n"
    "v_mfma_f32_16x16x32_bf16 a[124:127], v[60:63], v[92:95], a[124:127]\n"
    "ds_read_b128 v[60:63], %[vA2] offset:7296\n"
    "s_waitcnt vmcnt(8) lgkmcnt(6)\n"
    "v_mfma_f32_16x16x32_bf16 a[0:3], v[32:35], v[96:99], a[0:3]\n"
    "v_mfma_f32_16x16x32_bf16 a[4:7], v[32:35], v[100:103], a[4:7]\n"
    "v_mfma_f32_16x16x32_bf16 a[8:11], v[32:35], v[104:107], a[8:11]\n"
    "v_mfma_f32_16x16x32_bf16 a[12:15], v[32:35], v[108:111], a[12:15]\n"
    "ds_read_b128 v[32:35], %[vA0] offset:8448\n"
    "v_mfma_f32_16x16x32_bf16 a[16:19], v[36:39], v[96:99], a[16:19]\n"
    "v_mfma_f32_16x16x32_bf16 a[20:23], v[36:39], v[100:103], a[20:23]\n"
    "buffer_load_dwordx4 v[80:83], %[vW], s[60:63], s68 offen offset:0 sc1\n"
    "v_mfma_f32_16x16x32_bf16 a[24:27], v[36:39], v[104:107], a[24:27]\n"
    "v_mfma_f32_16x16x32_bf16 a[28:31], v[36:39], v[108:111], a[28:31]\n"
    "ds_read_b128 v[36:39], %[vA0] offset:9472\n"
    "s_waitcnt lgkmcnt(6)\n"
    "v_mfma_f32_16x16x32_bf16 a[32:35], v[40:43], v[96:99], a[32:35]\n"
    "v_mfma_f32_16x16x32_bf16 a[36:39], v[40:43], v[100:103], a[36:39]\n"
    "v_mfma_f32_16x16x32_bf16 a[40:43], v[40:43], v[104:107], a[40:43]\n"
    "v_mfma_f32_16x16x32_bf16 a[44:47], v[40:43], v[108:111], a[44:47]\n"
    "ds_read_b128 v[40:43], %[vA0] offset:10496\n"
    "v_mfma_f32_16x16x32_bf16 a[48:51], v[44:47], v[96:99], a[48:51]\n"
    "v_mfma_f32_16x16x32_bf16 a[52:55], v[44:47], v[100:103], a[52:55]\n"
    "buffer_load_dwordx4 v[84:87], %[vW], s[60:63], s68 offen offset:1024 sc1\n"
    "v_mfma_f32_16x16x32_bf16 a[56:59], v[44:47], v[104:107], a[56:59]\n"
    "v_mfma_f32_16x16x32_bf16 a[60:63], v[44:47], v[108:111], a[60:63]\n"
    "ds_read_b128 v[44:47], %[vA0] offset:11520\n"
    "s_waitcnt lgkmcnt(6)\n"
    "v_mfma_f32_16x16x32_bf16 a[64:67], v[48:51], v[96:99], a[64:67]\n"
    "v_mfma_f32_16x16x32_bf16 a[68:71], v[48:51], v[100:103], a[68:71]\n"
    "v_mfma_f32_16x16x32_bf16 a[72:75], v[48:51], v[104:107], a[72:75]\n"
    "v_mfma_f32_16x16x32_bf16 a[76:79], v[48:51], v[108:111], a[76:79]\n"
    "ds_read_b128 v[48:51], %[vA0] offset:12672\n"
    "v_mfma_f32_16x16x32_bf16 a[80:83], v[52:55], v[96:99], a[80:83]\n"
    "v_mfma_f32_16x16x32_bf16 a[84:87], v[52:55], v[100:103], a[84:87]\n"
    "buffer_load_dwordx4 v[88:91], %[vW], s[60:63], s68 offen offset:2048 sc1\n"
    "v_mfma_f32_16x16x32_bf16 a[88:91], v[52:55], v[104:107], a[88:91]\n"
    "v_mfma_f32_16x16x32_bf16 a[92:95], v[52:55], v[108:111], a[92:95]\n"
    "ds_read_b128 v[52:55], %[vA0] offset:13696\n"
    "s_waitcnt lgkmcnt(6)\n"
    "v_mfma_f32_16x16x32_bf16 a[96:99], v[56:59], v[96:99], a[96:99]\n"
    "v_mfma_f32_16x16x32_bf16 a[100:103], v[56:59], v[100:103], a[100:103]\n"
    "v_mfma_f32_16x16x32_bf16 a[104:107], v[56:59], v[104:107], a[104:107]\n"
    "buffer_load_dwordx4 v[92:95], %[vW], s[60:63], s68 offen offset:3072 sc1\n"
    "v_mfma_f32_16x16x32_bf16 a[108:111], v[56:59], v[108:111], a[108:111]\n"
    "ds_read_b128 v[56:59], %[vA0] offset:14720\n"
    "v_mfma_f32_16x16x32_bf16 a[112:115], v[60:63], v[96:99], a[112:115]\n"
    "s_mov_b32 s68, %[ust2]\n"
    "v_mfma_f32_16x16x32_bf16 a[116:119], v[60:63], v[100:103], a[116:119]\n"
    "v_mfma_f32_16x16x32_bf16 a[120:123], v[60:63], v[104:107], a[120:123]\n"
    "v_mfma_f32_16x16x32_bf16 a[124:127], v[60:63], v[108:111], a[124:127]\n"
    "ds_read_b128 v[60:63], %[vA0] offset:15744\n"
    "s_waitcnt vmcnt(8) lgkmcnt(6)\n"
    "v_mfma_f32_16x16x32_bf16 a[0:3], v[32:35], v[112:115], a[0:3]\n"
    "v_mfma_f32_16x16x32_bf16 a[4:7], v[32:35], v[116:119], a[4:7]\n"
    "v_mfma_f32_16x16x32_bf16 a[8:11], v[32:35], v[120:123], a[8:11]\n"
    "v_mfma_f32_16x16x32_bf16 a[12:15], v[32:35], v[124:127], a[12:15]\n"
    "ds_read_b128 v[32:35], %[vA1] offset:8448\n"
    "v_mfma_f32_16x16x32_bf16 a[16:19], v[36:39], v[112:115], a[16:19]\n"
    "v_mfma_f32_16x16x32_bf16 a[20:23], v[36:39], v[116:119], a[20:23]\n"
    "buffer_load_dwordx4 v[96:99], %[vW], s[60:63], s68 offen offset:0 sc1\n"
    "v_mfma_f32_16x16x32_bf16 a[24:27], v[36:39], v[120:123], a[24:27]\n"
    "v_mfma_f32_16x16x32_bf16 a[28:31], v[36:39], v[124:127], a[28:31]\n"
    "ds_read_b128 v[36:39], %[vA1] offset:9472\n"
    "s_waitcnt lgkmcnt(6)\n"
    "v_mfma_f32_16x16x32_bf16 a[32:35], v[40:43], v[112:115], a[32:35]\n"
    "v_mfma_f32_16x16x32_bf16 a[36:39], v[40:43], v[116:119], a[36:39]\n"
    "v_mfma_f32_16x16x32_bf16 a[40:43], v[40:43], v[120:123], a[40:43]\n"
    "v_mfma_f32_16x16x32_bf16 a[44:47], v[40:43], v[124:127], a[44:47]\n"
    "ds_read_b128 v[40:43], %[vA1] offset:10496\n"
    "v_mfma_f32_16x16x32_bf16 a[48:51], v[44:47], v[112:115], a[48:51]\n"
    "v_mfma_f32_16x16x32_bf16 a[52:55], v[44:47], v[116:119], a[52:55]\n"
    "buffer_load_dwordx4 v[100:103], %[vW], s[60:63], s68 offen offset:1024 sc1\n"
    "v_mfma_f32_16x16x32_bf16 a[56:59], v[44:47], v[120:123], a[56:59]\n"
    "v_mfma_f32_16x16x32_bf16 a[60:63], v[44:47], v[124:127], a[60:63]\n"
    "ds_read_b128 v[44:47], %[vA1] offset:11520\n"
    "s_waitcnt lgkmcnt(6)\n"
    "v_mfma_f32_16x16x32_bf16 a[64:67], v[48:51], v[112:115], a[64:67]\n"
    "v_mfma_f32_16x16x32_bf16 a[68:71], v[48:51], v[116:119], a[68:71]\n"
    "v_mfma_f32_16x16x32_bf16 a[72:75], v[48:51], v[120:123], a[72:75]\n"
    "v_mfma_f32_16x16x32_bf16 a[76:79], v[48:51], v[124:127], a[76:79]\n"
    "ds_read_b128 v[48:51], %[vA1] offset:12672\n"
    "v_mfma_f32_16x16x32_bf16 a[80:83], v[52:55], v[112:115], a[80:83]\n"
    "v_mfma_f32_16x16x32_bf16 a[84:87], v[52:55], v[116:119], a[84:87]\n"
    "buffer_load_dwordx4 v[104:107], %[vW], s[60:63], s68 offen offset:2048 sc1\n"
    "v_mfma_f32_16x16x32_bf16 a[88:91], v[52:55], v[120:123], a[88:91]\n"
    "v_mfma_f32_16x16x32_bf16 a[92:95], v[52:55], v[124:127], a[92:95]\n"
    "ds_read_b128 v[52:55], %[vA1] offset:13696\n"
    "s_waitcnt lgkmcnt(6)\n"
    "v_mfma_f32_16x16x32_bf16 a[96:99], v[56:59], v[112:115], a[96:99]\n"
    "v_mfma_f32_16x16x32_bf16 a[100:103], v[56:59], v[116:119], a[100:103]\n"
    "v_mfma_f32_16x16x32_bf16 a[104:107], v[56:59], v[120:123], a[104:107]\n"
    "buffer_load_dwordx4 v[108:111], %[vW], s[60:63], s68 offen offset:3072 sc1\n"
    "v_mfma_f32_16x16x32_bf16 a[108:111], v[56:59], v[124:127], a[108:111]\n"
    "ds_read_b128 v[56:59], %[vA1] offset:14720\n"
    "v_mfma_f32_16x16x32_bf16 a[112:115], v[60:63], v[112:115], a[112:115]\n"
    "s_sub_u32 s74, s74, 1\n"
    "s_cselect_b32 s73, s77, s76\n"
    "v_mfma_f32_16x16x32_bf16 a[116:119], v[60:63], v[116:119], a[116:119]\n"
    "s_add_u32 s68, s68, s73\n"
    "v_mfma_f32_16x16x32_bf16 a[120:123], v[60:63], v[120:123], a[120:123]\n"
    "v_mfma_f32_16x16x32_bf16 a[124:127], v[60:63], v[124:127], a[124:127]\n"
    "ds_read_b128 v[60:63], %[vA1] offset:15744\n"
    "s_waitcnt vmcnt(8) lgkmcnt(6)\n"
    "v_mfma_f32_16x16x32_bf16 a[0:3], v[32:35], v[64:67], a[0:3]\n"
    "v_mfma_f32_16x16x32_bf16 a[4:7], v[32:35], v[68:71], a[4:7]\n"
    "v_mfma_f32_16x16x32_bf16 a[8:11], v[32:35], v[72:75], a[8:11]\n"
    "v_mfma_f32_16x16x32_bf16 a[12:15], v[32:35], v[76:79], a[12:15]\n"
    "ds_read_b128 v[32:35], %[vA2] offset:8448\n"
    "v_mfma_f32_16x16x32_bf16 a[16:19], v[36:39], v[64:67], a[16:19]\n"
    "v_mfma_f32_16x16x32_bf16 a[20:23], v[36:39], v[68:71], a[20:23]\n"
    "buffer_load_dwordx4 v[112:115], %[vW], s[60:63], s68 offen offset:0 sc1\n"
    "v_mfma_f32_16x16x32_bf16 a[24:27], v[36:39], v[72:75], a[24:27]\n"
    "v_mfma_f32_16x16x32_bf16 a[28:31], v[36:39], v[76:79], a[28:31]\n"
    "ds_read_b128 v[36:39], %[vA2] offset:9472\n"
    "s_waitcnt lgkmcnt(6)\n"
    "v_mfma_f32_16x16x32_bf16 a[32:35], v[40:43], v[64:67], a[32:35]\n"
    "v_mfma_f32_16x16x32_bf16 a[36:39], v[40:43], v[68:71], a[36:39]\n"
    "v_mfma_f32_16x16x32_bf16 a[40:43], v[40:43], v[72:75], a[40:43]\n"
    "v_mfma_f32_16x16x32_bf16 a[44:47], v[40:43], v[76:79], a[44:47]\n"
    "ds_read_b128 v[40:43], %[vA2] offset:10496\n"
    "v_mfma_f32_16x16x32_bf16 a[48:51], v[44:47], v[64:67], a[48:51]\n"
    "v_mfma_f32_16x16x32_bf16 a[52:55], v[44:47], v[68:71], a[52:55]\n"
    "buffer_load_dwordx4 v[116:119], %[vW], s[60:63], s68 offen offset:1024 sc1\n"
    "v_mfma_f32_16x16x32_bf16 a[56:59], v[44:47], v[72:75], a[56:59]\n"
    "v_mfma_f32_16x16x32_bf16 a[60:63], v[44:47], v[76:79], a[60:63]\n"
    "ds_read_b128 v[44:47], %[vA2] offset:11520\n"
    "s_waitcnt lgkmcnt(6)\n"
    "v_mfma_f32_16x16x32_bf16 a[64:67], v[48:51], v[64:67], a[64:67]\n"
    "v_mfma_f32_16x16x32_bf16 a[68:71], v[48:51], v[68:71], a[68:71]\n"
    "v_mfma_f32_16x16x32_bf16 a[72:75], v[48:51], v[72:75], a[72:75]\n"
    "v_mfma_f32_16x16x32_bf16 a[76:79], v[48:51], v[76:79], a[76:79]\n"
    "ds_read_b128 v[48:51], %[vA2] offset:12672\n"
    "v_mfma_f32_16x16x32_bf16 a[80:83], v[52:55], v[64:67], a[80:83]\n"
    "v_mfma_f32_16x16x32_bf16 a[84:87], v[52:55], v[68:71], a[84:87]\n"
    "buffer_load_dwordx4 v[120:123], %[vW], s[60:63], s68 offen offset:2048 sc1\n"
    "v_mfma_f32_16x16x32_bf16 a[88:91], v[52:55], v[72:75], a[88:91]\n"
    "v_mfma_f32_16x16x32_bf16 a[92:95], v[52:55], v[76:79], a[92:95]\n"
    "ds_read_b128 v[52:55], %[vA2] offset:13696\n"
    "s_waitcnt lgkmcnt(6)\n"
    "v_mfma_f32_16x16x32_bf16 a[96:99], v[56:59], v[64:67], a[96:99]\n"
    "v_mfma_f32_16x16x32_bf16 a[100:103], v[56:59], v[68:71], a[100:103]\n"
    "v_mfma_f32_16x16x32_bf16 a[104:107], v[56:59], v[72:75], a[104:107]\n"
    "buffer_load_dwordx4 v[124:127], %[vW], s[60:63], s68 offen offset:3072 sc1\n"
    "v_mfma_f32_16x16x32_bf16 a[108:111], v[56:59], v[76:79], a[108:111]\n"
    "ds_read_b128 v[56:59], %[vA2] offset:14720\n"
    "v_mfma_f32_16x16x32_bf16 a[112:115], v[60:63], v[64:67], a[112:115]\n"
    "s_sub_u32 s74, s74, 1\n"
    "s_cselect_b32 s73, s77, s76\n"
    "v_mfma_f32_16x16x32_bf16 a[116:119], v[60:63], v[68:71], a[116:119]\n"
    "s_add_u32 s68, s68, s73\n"
    "v_mfma_f32_16x16x32_bf16 a[120:123], v[60:63], v[72:75], a[120:123]\n"
    "v_mfma_f32_16x16x32_bf16 a[124:127], v[60:63], v[76:79], a[124:127]\n"
    "ds_read_b128 v[60:63], %[vA2] offset:15744\n"
    "s_waitcnt vmcnt(8) lgkmcnt(6)\n"
    "v_mfma_f32_16x16x32_bf16 a[0:3], v[32:35], v[80:83], a[0:3]\n"
    "v_mfma_f32_16x16x32_bf16 a[4:7], v[32:35], v[84:87], a[4:7]\n"
    "v_mfma_f32_16x16x32_bf16 a[8:11], v[32:35], v[88:91], a[8:11]\n"
    "v_mfma_f32_16x16x32_bf16 a[12:15], v[32:35], v[92:95], a[12:15]\n"
    "v_mfma_f32_16x16x32_bf16 a[16:19], v[36:39], v[80:83], a[16:19]\n"
    "v_mfma_f32_16x16x32_bf16 a[20:23], v[36:39], v[84:87], a[20:23]\n"
    "buffer_load_dwordx4 v[64:67], %[vW], s[60:63], s68 offen offset:0 sc1\n"
    "v_mfma_f32_16x16x32_bf16 a[24:27], v[36:39], v[88:91], a[24:27]\n"
    "v_mfma_f32_16x16x32_bf16 a[28:31], v[36:39], v[92:95], a[28:31]\n"
    "s_waitcnt lgkmcnt(4)\n"
    "v_mfma_f32_16x16x32_bf16 a[32:35], v[40:43], v[80:83], a[32:35]\n"
    "v_mfma_f32_16x16x32_bf16 a[36:39], v[40:43], v[84:87], a[36:39]\n"
    "v_mfma_f32_16x16x32_bf16 a[40:43], v[40:43], v[88:91], a[40:43]\n"
    "v_mfma_f32_16x16x32_bf16 a[44:47], v[40:43], v[92:95], a[44:47]\n"
    "v_mfma_f32_16x16x32_bf16 a[48:51], v[44:47], v[80:83], a[48:51]\n"
    "v_mfma_f32_16x16x32_bf16 a[52:55], v[44:47], v[84:87], a[52:55]\n"
    "buffer_load_dwordx4 v[68:71], %[vW], s[60:63], s68 offen offset:1024 sc1\n"
    "v_mfma_f32_16x16x32_bf16 a[56:59], v[44:47], v[88:91], a[56:59]\n"
    "v_mfma_f32_16x16x32_bf16 a[60:63], v[44:47], v[92:95], a[60:63]\n"
    "s_waitcnt lgkmcnt(2)\n"
    "v_mfma_f32_16x16x32_bf16 a[64:67], v[48:51], v[80:83], a[64:67]\n"
    "v_mfma_f32_16x16x32_bf16 a[68:71], v[48:51], v[84:87], a[68:71]\n"
    "v_mfma_f32_16x16x32_bf16 a[72:75], v[48:51], v[88:91], a[72:75]\n"
    "v_mfma_f32_16x16x32_bf16 a[76:79], v[48:51], v[92:95], a[76:79]\n"
    "v_mfma_f32_16x16x32_bf16 a[80:83], v[52:55], v[80:83], a[80:83]\n"
    "v_mfma_f32_16x16x32_bf16 a[84:87], v[52:55], v[84:87], a[84:87]\n"
    "buffer_load_dwordx4 v[72:75], %[vW], s[60:63], s68 offen offset:2048 sc1\n"
    "v_mfma_f32_16x16x32_bf16 a[88:91], v[52:55], v[88:91], a[88:91]\n"
    "v_mfma_f32_16x16x32_bf16 a[92:95], v[52:55], v[92:95], a[92:95]\n"
    "s_waitcnt lgkmcnt(0)\n"
    "v_mfma_f32_16x16x32_bf16 a[96:99], v[56:59], v[80:83], a[96:99]\n"
    "v_mfma_f32_16x16x32_bf16 a[100:103], v[56:59], v[84:87], a[100:103]\n"
    "v_mfma_f32_16x16x32_bf16 a[104:107], v[56:59], v[88:91], a[104:107]\n"
    "buffer_load_dwordx4 v[76:79], %[vW], s[60:63], s68 offen offset:3072 sc1\n"
    "v_mfma_f32_16x16x32_bf16 a[108:111], v[56:59], v[92:95], a[108:111]\n"
    "s_sub_u32 s74, s74, 1\n"
    "s_cselect_b32 s73, s77, s76\n"
    "v_mfma_f32_16x16x32_bf16 a[112:115], v[60:63], v[80:83], a[112:115]\n"
    "s_add_u32 s68, s68, s73\n"
    "v_mfma_f32_16x16x32_bf16 a[116:119], v[60:63], v[84:87], a[116:119]\n"
    "v_mfma_f32_16x16x32_bf16 a[120:123], v[60:63], v[88:91], a[120:123]\n"
    "v_mfma_f32_16x16x32_bf16 a[124:127], v[60:63], v[92:95], a[124:127]\n"
    "s_mov_b32 s72, 0\n"
    "POLL%=:\n"
    "ds_read_b32 v31, %[vRdy]\n"
    "s_waitcnt lgkmcnt(0)\n"
    "v_cmp_ne_u32 vcc, 0, v31\n"
    "s_cbranch_vccnz PDONE%=\n"
    "s_add_u32 s72, s72, 1\n"
    "s_cmp_gt_u32 s72, 4194304\n"
    "s_cbranch_scc1 PDONE%=\n"
    "s_branch POLL%=\n"
    "PDONE%=:\n"
    "ds_read_b128 v[32:35], %[vA0] offset:16896\n"
    "ds_read_b128 v[36:39], %[vA0] offset:17920\n"
    "ds_read_b128 v[40:43], %[vA0] offset:18944\n"
    "ds_read_b128 v[44:47], %[vA0] offset:19968\n"
    "ds_read_b128 v[48:51], %[vA0] offset:21120\n"
    "ds_read_b128 v[52:55], %[vA0] offset:22144\n"
    "ds_read_b128 v[56:59], %[vA0] offset:23168\n"
    "ds_read_b128 v[60:63], %[vA0] offset:24192\n"
    "s_add_u32 m0, %[mb], 64\n"
    "s_add_u32 s70, s69, 0\n"
    "buffer_load_dwordx4 %[vW], s[64:67], s70 offen sc1 lds\n"
    "s_add_u32 m0, %[mb], 1088\n"
    "s_add_u32 s70, s69, 1024\n"
    "buffer_load_dwordx4 %[vW], s[64:67], s70 offen sc1 lds\n"
    "s_add_u32 m0, %[mb], 2112\n"
    "s_add_u32 s70, s69, 2048\n"
    "buffer_load_dwordx4 %[vW], s[64:67], s70 offen sc1 lds\n"
    "s_add_u32 m0, %[mb], 3136\n"
    "s_add_u32 s70, s69, 3072\n"
    "buffer_load_dwordx4 %[vW], s[64:67], s70 offen sc1 lds\n"
    "s_add_u32 m0, %[mb], 4288\n"
    "s_add_u32 s70, s69, 262144\n"
    "buffer_load_dwordx4 %[vW], s[64:67], s70 offen sc1 lds\n"
    "s_add_u32 m0, %[mb], 5312\n"
    "s_add_u32 s70, s69, 263168\n"
    "buffer_load_dwordx4 %[vW], s[64:67], s70 offen sc1 lds\n"
    "s_add_u32 m0, %[mb], 6336\n"
    "s_add_u32 s70, s69, 264192\n"
    "buffer_load_dwordx4 %[vW], s[64:67], s70 offen sc1 lds\n"
    "s_add_u32 m0, %[mb], 7360\n"
    "s_add_u32 s70, s69, 265216\n"
    "buffer_load_dwordx4 %[vW], s[64:67], s70 offen sc1 lds\n"
    "s_sub_u32 s75, s75, 1\n"
    "s_cselect_b32 s79, s78, s76\n"
    "s_add_u32 s69, s69, s79\n"
    "LOOP%=:\n"
    "s_waitcnt vmcnt(13) lgkmcnt(6)\n"
    "v_mfma_f32_16x16x32_bf16 a[0:3], v[32:35], v[96:99], a[0:3]\n"
    "s_add_u32 s70, s69, 0\n"
    "v_mfma_f32_16x16x32_bf16 a[4:7], v[32:35], v[100:103], a[4:7]\n"
    "s_add_u32 m0, %[mb], 8512\n"
    "v_mfma_f32_16x16x32_bf16 a[8:11], v[32:35], v[104:107], a[8:11]\n"
    "buffer_load_dwordx4 %[vW], s[64:67], s70 offen sc1 lds\n"
    "v_mfma_f32_16x16x32_bf16 a[12:15], v[32:35], v[108:111], a[12:15]\n"
    "ds_read_b128 v[32:35], %[vA1] offset:16896\n"
    "v_mfma_f32_16x16x32_bf16 a[16:19], v[36:39], v[96:99], a[16:19]\n"
    "v_mfma_f32_16x16x32_bf16 a[20:23], v[36:39], v[100:103], a[20:23]\n"
    "buffer_load_dwordx4 v[80:83], %[vW], s[60:63], s68 offen offset:0 sc1\n"
    "v_mfma_f32_16x16x32_bf16 a[24:27], v[36:39], v[104:107], a[24:27]\n"
    "v_mfma_f32_16x16x32_bf16 a[28:31], v[36:39], v[108:111], a[28:31]\n"
    "ds_read_b128 v[36:39], %[vA1] offset:17920\n"
    "s_waitcnt lgkmcnt(6)\n"
    "v_mfma_f32_16x16x32_bf16 a[32:35], v[40:43], v[96:99], a[32:35]\n"
    "v_mfma_f32_16x16x32_bf16 a[36:39], v[40:43], v[100:103], a[36:39]\n"
    "v_mfma_f32_16x16x32_bf16 a[40:43], v[40:43], v[104:107], a[40:43]\n"
    "s_add_u32 s70, s69, 1024\n"
    "v_mfma_f32_16x16x32_bf16 a[44:47], v[40:43], v[108:111], a[44:47]\n"
    "ds_read_b128 v[40:43], %[vA1] offset:18944\n"
    "v_mfma_f32_16x16x32_bf16 a[48:51], v[44:47], v[96:99], a[48:51]\n"
    "s_add_u32 m0, %[mb], 9536\n"
    "v_mfma_f32_16x16x32_bf16 a[52:55], v[44:47], v[100:103], a[52:55]\n"
    "buffer_load_dwordx4 %[vW], s[64:67], s70 offen sc1 lds\n"
    "v_mfma_f32_16x16x32_bf16 a[56:59], v[44:47], v[104:107], a[56:59]\n"
    "buffer_load_dwordx4 v[84:87], %[vW], s[60:63], s68 offen offset:1024 sc1\n"
    "v_mfma_f32_16x16x32_bf16 a[60:63], v[44:47], v[108:111], a[60:63]\n"
    "ds_read_b128 v[44:47], %[vA1] offset:19968\n"
    "s_waitcnt lgkmcnt(6)\n"
    "v_mfma_f32_16x16x32_bf16 a[64:67], v[48:51], v[96:99], a[64:67]\n"
    "v_mfma_f32_16x16x32_bf16 a[68:71], v[48:51], v[100:103], a[68:71]\n"
    "v_mfma_f32_16x16x32_bf16 a[72:75], v[48:51], v[104:107], a[72:75]\n"
    "v_mfma_f32_16x16x32_bf16 a[76:79], v[48:51], v[108:111], a[76:79]\n"
    "ds_read_b128 v[48:51], %[vA1] offset:21120\n"
    "v_mfma_f32_16x16x32_bf16 a[80:83], v[52:55], v[96:99], a[80:83]\n"
    "s_add_u32 s70, s69, 2048\n"
    "v_mfma_f32_16x16x32_bf16 a[84:87], v[52:55], v[100:103], a[84:87]\n"
    "s_add_u32 m0, %[mb], 10560\n"
    "v_mfma_f32_16x16x32_bf16 a[88:91], v[52:55], v[104:107], a[88:91]\n"
    "buffer_load_dwordx4 %[vW], s[64:67], s70 offen sc1 lds\n"
    "v_mfma_f32_16x16x32_bf16 a[92:95], v[52:55], v[108:111], a[92:95]\n"
    "ds_read_b128 v[52:55], %[vA1] offset:22144\n"
    "s_waitcnt lgkmcnt(6)\n"
    "v_mfma_f32_16x16x32_bf16 a[96:99], v[56:59], v[96:99], a[96:99]\n"
    "buffer_load_dwordx4 v[88:91], %[vW], s[60:63], s68 offen offset:2048 sc1\n"
    "v_mfma_f32_16x16x32_bf16 a[100:103], v[56:59], v[100:103], a[100:103]\n"
    "v_mfma_f32_16x16x32_bf16 a[104:107], v[56:59], v[104:107], a[104:107]\n"
    "buffer_load_dwordx4 v[92:95], %[vW], s[60:63], s68 offen offset:3072 sc1\n"
    "v_mfma_f32_16x16x32_bf16 a[108:111], v[56:59], v[108:111], a[108:111]\n"
    "ds_read_b128 v[56:59], %[vA1] offset:23168\n"
    "v_mfma_f32_16x16x32_bf16 a[112:115], v[60:63], v[96:99], a[112:115]\n"
    "s_sub_u32 s74, s74, 1\n"
    "s_cselect_b32 s73, s77, s76\n"
    "v_mfma_f32_16x16x32_bf16 a[116:119], v[60:63], v[100:103], a[116:119]\n"
    "s_add_u32 s68, s68, s73\n"
    "v_mfma_f32_16x16x32_bf16 a[120:123], v[60:63], v[104:107], a[120:123]\n"
    "v_mfma_f32_16x16x32_bf16 a[124:127], v[60:63], v[108:111], a[124:127]\n"
    "ds_read_b128 v[60:63], %[vA1] offset:24192\n"
    "s_waitcnt vmcnt(13) lgkmcnt(6)\n"
    "v_mfma_f32_16x16x32_bf16 a[0:3], v[32:35], v[112:115], a[0:3]\n"
    "s_add_u32 s70, s69, 3072\n"
    "v_mfma_f32_16x16x32_bf16 a[4:7], v[32:35], v[116:119], a[4:7]\n"
    "s_add_u32 m0, %[mb], 11584\n"
    "v_mfma_f32_16x16x32_bf16 a[8:11], v[32:35], v[120:123], a[8:11]\n"
    "buffer_load_dwordx4 %[vW], s[64:67], s70 offen sc1 lds\n"
    "v_mfma_f32_16x16x32_bf16 a[12:15], v[32:35], v[124:127], a[12:15]\n"
    "ds_read_b128 v[32:35], %[vA2] offset:16896\n"
    "v_mfma_f32_16x16x32_bf16 a[16:19], v[36:39], v[112:115], a[16:19]\n"
    "v_mfma_f32_16x16x32_bf16 a[20:23], v[36:39], v[116:119], a[20:23]\n"
    "buffer_load_dwordx4 v[96:99], %[vW], s[60:63], s68 offen offset:0 sc1\n"
    "v_mfma_f32_16x16x32_bf16 a[24:27], v[36:39], v[120:123], a[24:27]\n"
    "v_mfma_f32_16x16x32_bf16 a[28:31], v[36:39], v[124:127], a[28:31]\n"
    "ds_read_b128 v[36:39], %[vA2] offset:17920\n"
    "s_waitcnt lgkmcnt(6)\n"
    "v_mfma_f32_16x16x32_bf16 a[32:35], v[40:43], v[112:115], a[32:35]\n"
    "v_mfma_f32_16x16x32_bf16 a[36:39], v[40:43], v[116:119], a[36:39]\n"
    "v_mfma_f32_16x16x32_bf16 a[40:43], v[40:43], v[120:123], a[40:43]\n"
    "s_add_u32 s70, s69, 262144\n"
    "v_mfma_f32_16x16x32_bf16 a[44:47], v[40:43], v[124:127], a[44:47]\n"
    "ds_read_b128 v[40:43], %[vA2] offset:18944\n"
    "v_mfma_f32_16x16x32_bf16 a[48:51], v[44:47], v[112:115], a[48:51]\n"
    "s_add_u32 m0, %[mb], 12736\n"
    "v_mfma_f32_16x16x32_bf16 a[52:55], v[44:47], v[116:119], a[52:55]\n"
    "buffer_load_dwordx4 %[vW], s[64:67], s70 offen sc1 lds\n"
    "v_mfma_f32_16x16x32_bf16 a[56:59], v[44:47], v[120:123], a[56:59]\n"
    "buffer_load_dwordx4 v[100:103], %[vW], s[60:63], s68 offen offset:1024 sc1\n"
    "v_mfma_f32_16x16x32_bf16 a[60:63], v[44:47], v[124:127], a[60:63]\n"
    "ds_read_b128 v[44:47], %[vA2] offset:19968\n"
    "s_waitcnt lgkmcnt(6)\n"
    "v_mfma_f32_16x16x32_bf16 a[64:67], v[48:51], v[112:115], a[64:67]\n"
    "v_mfma_f32_16x16x32_bf16 a[68:71], v[48:51], v[116:119], a[68:71]\n"
    "v_mfma_f32_16x16x32_bf16 a[72:75], v[48:51], v[120:123], a[72:75]\n"
    "v_mfma_f32_16x16x32_bf16 a[76:79], v[48:51], v[124:127], a[76:79]\n"
    "ds_read_b128 v[48:51], %[vA2] offset:21120\n"
    "v_mfma_f32_16x16x32_bf16 a[80:83], v[52:55], v[112:115], a[80:83]\n"
    "s_add_u32 s70, s69, 263168\n"
    "v_mfma_f32_16x16x32_bf16 a[84:87], v[52:55], v[116:119], a[84:87]\n"
    "s_add_u32 m0, %[mb], 13760\n"
    "v_mfma_f32_16x16x32_bf16 a[88:91], v[52:55], v[120:123], a[88:91]\n"
    "buffer_load_dwordx4 %[vW], s[64:67], s70 offen sc1 lds\n"
    "v_mfma_f32_16x16x32_bf16 a[92:95], v[52:55], v[124:127], a[92:95]\n"
    "ds_read_b128 v[52:55], %[vA2] offset:22144\n"
    "s_waitcnt lgkmcnt(6)\n"
    "v_mfma_f32_16x16x32_bf16 a[96:99], v[56:59], v[112:115], a[96:99]\n"
    "buffer_load_dwordx4 v[104:107], %[vW], s[60:63], s68 offen offset:2048 sc1\n"
    "v_mfma_f32_16x16x32_bf16 a[100:103], v[56:59], v[116:119], a[100:103]\n"
    "v_mfma_f32_16x16x32_bf16 a[104:107], v[56:59], v[120:123], a[104:107]\n"
    "buffer_load_dwordx4 v[108:111], %[vW], s[60:63], s68 offen offset:3072 sc1\n"
    "v_mfma_f32_16x16x32_bf16 a[108:111], v[56:59], v[124:127], a[108:111]\n"
    "ds_read_b128 v[56:59], %[vA2] offset:23168\n"
    "v_mfma_f32_16x16x32_bf16 a[112:115], v[60:63], v[112:115], a[112:115]\n"
    "s_sub_u32 s74, s74, 1\n"
    "s_cselect_b32 s73, s77, s76\n"
    "v_mfma_f32_16x16x32_bf16 a[116:119], v[60:63], v[116:119], a[116:119]\n"
    "s_add_u32 s68, s68, s73\n"
    "v_mfma_f32_16x16x32_bf16 a[120:123], v[60:63], v[120:123], a[120:123]\n"
    "v_mfma_f32_16x16x32_bf16 a[124:127], v[60:63], v[124:127], a[124:127]\n"
    "ds_read_b128 v[60:63], %[vA2] offset:24192\n"
    "s_waitcnt vmcnt(14) lgkmcnt(6)\n"
    "v_mfma_f32_16x16x32_bf16 a[0:3], v[32:35], v[64:67], a[0:3]\n"
    "s_add_u32 s70, s69, 264192\n"
    "v_mfma_f32_16x16x32_bf16 a[4:7], v[32:35], v[68:71], a[4:7]\n"
    "s_add_u32 m0, %[mb], 14784\n"
    "v_mfma_f32_16x16x32_bf16 a[8:11], v[32:35], v[72:75], a[8:11]\n"
    "buffer_load_dwordx4 %[vW], s[64:67], s70 offen sc1 lds\n"
    "v_mfma_f32_16x16x32_bf16 a[12:15], v[32:35], v[76:79], a[12:15]\n"
    "ds_read_b128 v[32:35], %[vA0] offset:25344\n"
    "v_mfma_f32_16x16x32_bf16 a[16:19], v[36:39], v[64:67], a[16:19]\n"
    "v_mfma_f32_16x16x32_bf16 a[20:23], v[36:39], v[68:71], a[20:23]\n"
    "buffer_load_dwordx4 v[112:115], %[vW], s[60:63], s68 offen offset:0 sc1\n"
    "v_mfma_f32_16x16x32_bf16 a[24:27], v[36:39], v[72:75], a[24:27]\n"
    "v_mfma_f32_16x16x32_bf16 a[28:31], v[36:39], v[76:79], a[28:31]\n"
    "ds_read_b128 v[36:39], %[vA0] offset:26368\n"
    "s_waitcnt lgkmcnt(6)\n"
    "v_mfma_f32_16x16x32_bf16 a[32:35], v[40:43], v[64:67], a[32:35]\n"
    "v_mfma_f32_16x16x32_bf16 a[36:39], v[40:43], v[68:71], a[36:39]\n"
    "v_mfma_f32_16x16x32_bf16 a[40:43], v[40:43], v[72:75], a[40:43]\n"
    "v_mfma_f32_16x16x32_bf16 a[44:47], v[40:43], v[76:79], a[44:47]\n"
    "ds_read_b128 v[40:43], %[vA0] offset:27392\n"
    "v_mfma_f32_16x16x32_bf16 a[48:51], v[44:47], v[64:67], a[48:51]\n"
    "v_mfma_f32_16x16x32_bf16 a[52:55], v[44:47], v[68:71], a[52:55]\n"
    "buffer_load_dwordx4 v[116:119], %[vW], s[60:63], s68 offen offset:1024 sc1\n"
    "v_mfma_f32_16x16x32_bf16 a[56:59], v[44:47], v[72:75], a[56:59]\n"
    "v_mfma_f32_16x16x32_bf16 a[60:63], v[44:47], v[76:79], a[60:63]\n"
    "ds_read_b128 v[44:47], %[vA0] offset:28416\n"
    "s_waitcnt lgkmcnt(6)\n"
    "v_mfma_f32_16x16x32_bf16 a[64:67], v[48:51], v[64:67], a[64:67]\n"
    "s_add_u32 s70, s69, 265216\n"
    "v_mfma_f32_16x16x32_bf16 a[68:71], v[48:51], v[68:71], a[68:71]\n"
    "s_add_u32 m0, %[mb], 15808\n"
    "v_mfma_f32_16x16x32_bf16 a[72:75], v[48:51], v[72:75], a[72:75]\n"
    "buffer_load_dwordx4 %[vW], s[64:67], s70 offen sc1 lds\n"
    "v_mfma_f32_16x16x32_bf16 a[76:79], v[48:51], v[76:79], a[76:79]\n"
    "ds_read_b128 v[48:51], %[vA0] offset:29568\n"
    "v_mfma_f32_16x16x32_bf16 a[80:83], v[52:55], v[64:67], a[80:83]\n"
    "s_sub_u32 s75, s75, 1\n"
    "s_cselect_b32 s79, s78, s76\n"
    "v_mfma_f32_16x16x32_bf16 a[84:87], v[52:55], v[68:71], a[84:87]\n"
    "buffer_load_dwordx4 v[120:123], %[vW], s[60:63], s68 offen offset:2048 sc1\n"
    "v_mfma_f32_16x16x32_bf16 a[88:91], v[52:55], v[72:75], a[88:91]\n"
    "s_add_u32 s69, s69, s79\n"
    "v_mfma_f32_16x16x32_bf16 a[92:95], v[52:55], v[76:79], a[92:95]\n"
    "ds_read_b128 v[52:55], %[vA0] offset:30592\n"
    "s_waitcnt lgkmcnt(6)\n"
    "v_mfma_f32_16x16x32_bf16 a[96:99], v[56:59], v[64:67], a[96:99]\n"
    "v_mfma_f32_16x16x32_bf16 a[100:103], v[56:59], v[68:71], a[100:103]\n"
    "v_mfma_f32_16x16x32_bf16 a[104:107], v[56:59], v[72:75], a[104:107]\n"
    "buffer_load_dwordx4 v[124:127], %[vW], s[60:63], s68 offen offset:3072 sc1\n"
    "v_mfma_f32_16x16x32_bf16 a[108:111], v[56:59], v[76:79], a[108:111]\n"
    "ds_read_b128 v[56:59], %[vA0] offset:31616\n"
    "v_mfma_f32_16x16x32_bf16 a[112:115], v[60:63], v[64:67], a[112:115]\n"
    "s_sub_u32 s74, s74, 1\n"
    "s_cselect_b32 s73, s77, s76\n"
    "v_mfma_f32_16x16x32_bf16 a[116:119], v[60:63], v[68:71], a[116:119]\n"
    "s_add_u32 s68, s68, s73\n"
    "v_mfma_f32_16x16x32_bf16 a[120:123], v[60:63], v[72:75], a[120:123]\n"
    "v_mfma_f32_16x16x32_bf16 a[124:127], v[60:63], v[76:79], a[124:127]\n"
    "ds_read_b128 v[60:63], %[vA0] offset:32640\n"
    "s_waitcnt vmcnt(13) lgkmcnt(6)\n"
    "v_mfma_f32_16x16x32_bf16 a[0:3], v[32:35], v[80:83], a[0:3]\n"
    "s_add_u32 s70, s69, 0\n"
    "v_mfma_f32_16x16x32_bf16 a[4:7], v[32:35], v[84:87], a[4:7]\n"
    "s_add_u32 m0, %[mb], 16960\n"
    "v_mfma_f32_16x16x32_bf16 a[8:11], v[32:35], v[88:91], a[8:11]\n"
    "buffer_load_dwordx4 %[vW], s[64:67], s70 offen sc1 lds\n"
    "v_mfma_f32_16x16x32_bf16 a[12:15], v[32:35], v[92:95], a[12:15]\n"
    "ds_read_b128 v[32:35], %[vA1] offset:25344\n"
    "v_mfma_f32_16x16x32_bf16 a[16:19], v[36:39], v[80:83], a[16:19]\n"
    "v_mfma_f32_16x16x32_bf16 a[20:23], v[36:39], v[84:87], a[20:23]\n"
    "buffer_load_dwordx4 v[64:67], %[vW], s[60:63], s68 offen offset:0 sc1\n"
    "v_mfma_f32_16x16x32_bf16 a[24:27], v[36:39], v[88:91], a[24:27]\n"
    "v_mfma_f32_16x16x32_bf16 a[28:31], v[36:39], v[92:95], a[28:31]\n"
    "ds_read_b128 v[36:39], %[vA1] offset:26368\n"
    "s_waitcnt lgkmcnt(6)\n"
    "v_mfma_f32_16x16x32_bf16 a[32:35], v[40:43], v[80:83], a[32:35]\n"
    "v_mfma_f32_16x16x32_bf16 a[36:39], v[40:43], v[84:87], a[36:39]\n"
    "v_mfma_f32_16x16x32_bf16 a[40:43], v[40:43], v[88:91], a[40:43]\n"
    "s_add_u32 s70, s69, 1024\n"
    "v_mfma_f32_16x16x32_bf16 a[44:47], v[40:43], v[92:95], a[44:47]\n"
    "ds_read_b128 v[40:43], %[vA1] offset:27392\n"
    "v_mfma_f32_16x16x32_bf16 a[48:51], v[44:47], v[80:83], a[48:51]\n"
    "s_add_u32 m0, %[mb], 17984\n"
    "v_mfma_f32_16x16x32_bf16 a[52:55], v[44:47], v[84:87], a[52:55]\n"
    "buffer_load_dwordx4 %[vW], s[64:67], s70 offen sc1 lds\n"
    "v_mfma_f32_16x16x32_bf16 a[56:59], v[44:47], v[88:91], a[56:59]\n"
    "buffer_load_dwordx4 v[68:71], %[vW], s[60:63], s68 offen offset:1024 sc1\n"
    "v_mfma_f32_16x16x32_bf16 a[60:63], v[44:47], v[92:95], a[60:63]\n"
    "ds_read_b128 v[44:47], %[vA1] offset:28416\n"
    "s_waitcnt lgkmcnt(6)\n"
    "v_mfma_f32_16x16x32_bf16 a[64:67], v[48:51], v[80:83], a[64:67]\n"
    "v_mfma_f32_16x16x32_bf16 a[68:71], v[48:51], v[84:87], a[68:71]\n"
    "v_mfma_f32_16x16x32_bf16 a[72:75], v[48:51], v[88:91], a[72:75]\n"
    "v_mfma_f32_16x16x32_bf16 a[76:79], v[48:51], v[92:95], a[76:79]\n"
    "ds_read_b128 v[48:51], %[vA1] offset:29568\n"
    "v_mfma_f32_16x16x32_bf16 a[80:83], v[52:55], v[80:83], a[80:83]\n"
    "s_add_u32 s70, s69, 2048\n"
    "v_mfma_f32_16x16x32_bf16 a[84:87], v[52:55], v[84:87], a[84:87]\n"
    "s_add_u32 m0, %[mb], 19008\n"
    "v_mfma_f32_16x16x32_bf16 a[88:91], v[52:55], v[88:91], a[88:91]\n"
    "buffer_load_dwordx4 %[vW], s[64:67], s70 offen sc1 lds\n"
    "v_mfma_f32_16x16x32_bf16 a[92:95], v[52:55], v[92:95], a[92:95]\n"
    "ds_read_b128 v[52:55], %[vA1] offset:30592\n"
    "s_waitcnt lgkmcnt(6)\n"
    "v_mfma_f32_16x16x32_bf16 a[96:99], v[56:59], v[80:83], a[96:99]\n"
    "buffer_load_dwordx4 v[72:75], %[vW], s[60:63], s68 offen offset:2048 sc1\n"
    "v_mfma_f32_16x16x32_bf16 a[100:103], v[56:59], v[84:87], a[100:103]\n"
    "v_mfma_f32_16x16x32_bf16 a[104:107], v[56:59], v[88:91], a[104:107]\n"
    "buffer_load_dwordx4 v[76:79], %[vW], s[60:63], s68 offen offset:3072 sc1\n"
    "v_mfma_f32_16x16x32_bf16 a[108:111], v[56:59], v[92:95], a[108:111]\n"
    "ds_read_b128 v[56:59], %[vA1] offset:31616\n"
    "v_mfma_f32_16x16x32_bf16 a[112:115], v[60:63], v[80:83], a[112:115]\n"
    "s_sub_u32 s74, s74, 1\n"
    "s_cselect_b32 s73, s77, s76\n"
    "v_mfma_f32_16x16x32_bf16 a[116:119], v[60:63], v[84:87], a[116:119]\n"
    "s_add_u32 s68, s68, s73\n"
    "v_mfma_f32_16x16x32_bf16 a[120:123], v[60:63], v[88:91], a[120:123]\n"
    "v_mfma_f32_16x16x32_bf16 a[124:127], v[60:63], v[92:95], a[124:127]\n"
    "ds_read_b128 v[60:63], %[vA1] offset:32640\n"
    "s_waitcnt vmcnt(13) lgkmcnt(6)\n"
    "v_mfma_f32_16x16x32_bf16 a[0:3], v[32:35], v[96:99], a[0:3]\n"
    "s_add_u32 s70, s69, 3072\n"
    "v_mfma_f32_16x16x32_bf16 a[4:7], v[32:35], v[100:103], a[4:7]\n"
    "s_add_u32 m0, %[mb], 20032\n"
    "v_mfma_f32_16x16x32_bf16 a[8:11], v[32:35], v[104:107], a[8:11]\n"
    "buffer_load_dwordx4 %[vW], s[64:67], s70 offen sc1 lds\n"
    "v_mfma_f32_16x16x32_bf16 a[12:15], v[32:35], v[108:111], a[12:15]\n"
    "ds_read_b128 v[32:35], %[vA2] offset:25344\n"
    "v_mfma_f32_16x16x32_bf16 a[16:19], v[36:39], v[96:99], a[16:19]\n"
    "v_mfma_f32_16x16x32_bf16 a[20:23], v[36:39], v[100:103], a[20:23]\n"
    "buffer_load_dwordx4 v[80:83], %[vW], s[60:63], s68 offen offset:0 sc1\n"
    "v_mfma_f32_16x16x32_bf16 a[24:27], v[36:39], v[104:107], a[24:27]\n"
    "v_mfma_f32_16x16x32_bf16 a[28:31], v[36:39], v[108:111], a[28:31]\n"
    "ds_read_b128 v[36:39], %[vA2] offset:26368\n"
    "s_waitcnt lgkmcnt(6)\n"
    "v_mfma_f32_16x16x32_bf16 a[32:35], v[40:43], v[96:99], a[32:35]\n"
    "v_mfma_f32_16x16x32_bf16 a[36:39], v[40:43], v[100:103], a[36:39]\n"
    "v_mfma_f32_16x16x32_bf16 a[40:43], v[40:43], v[104:107], a[40:43]\n"
    "s_add_u32 s70, s69, 262144\n"
    "v_mfma_f32_16x16x32_bf16 a[44:47], v[40:43], v[108:111], a[44:47]\n"
    "ds_read_b128 v[40:43], %[vA2] offset:27392\n"
    "v_mfma_f32_16x16x32_bf16 a[48:51], v[44:47], v[96:99], a[48:51]\n"
    "s_add_u32 m0, %[mb], 21184\n"
    "v_mfma_f32_16x16x32_bf16 a[52:55], v[44:47], v[100:103], a[52:55]\n"
    "buffer_load_dwordx4 %[vW], s[64:67], s70 offen sc1 lds\n"
    "v_mfma_f32_16x16x32_bf16 a[56:59], v[44:47], v[104:107], a[56:59]\n"
    "buffer_load_dwordx4 v[84:87], %[vW], s[60:63], s68 offen offset:1024 sc1\n"
    "v_mfma_f32_16x16x32_bf16 a[60:63], v[44:47], v[108:111], a[60:63]\n"
    "ds_read_b128 v[44:47], %[vA2] offset:28416\n"
    "s_waitcnt lgkmcnt(6)\n"
    "v_mfma_f32_16x16x32_bf16 a[64:67], v[48:51], v[96:99], a[64:67]\n"
    "v_mfma_f32_16x16x32_bf16 a[68:71], v[48:51], v[100:103], a[68:71]\n"
    "v_mfma_f32_16x16x32_bf16 a[72:75], v[48:51], v[104:107], a[72:75]\n"
    "v_mfma_f32_16x16x32_bf16 a[76:79], v[48:51], v[108:111], a[76:79]\n"
    "ds_read_b128 v[48:51], %[vA2] offset:29568\n"
    "v_mfma_f32_16x16x32_bf16 a[80:83], v[52:55], v[96:99], a[80:83]\n"
    "s_add_u32 s70, s69, 263168\n"
    "v_mfma_f32_16x16x32_bf16 a[84:87], v[52:55], v[100:103], a[84:87]\n"
    "s_add_u32 m0, %[mb], 22208\n"
    "v_mfma_f32_16x16x32_bf16 a[88:91], v[52:55], v[104:107], a[88:91]\n"
    "buffer_load_dwordx4 %[vW], s[64:67], s70 offen sc1 lds\n"
    "v_mfma_f32_16x16x32_bf16 a[92:95], v[52:55], v[108:111], a[92:95]\n"
    "ds_read_b128 v[52:55], %[vA2] offset:30592\n"
    "s_waitcnt lgkmcnt(6)\n"
    "v_mfma_f32_16x16x32_bf16 a[96:99], v[56:59], v[96:99], a[96:99]\n"
    "buffer_load_dwordx4 v[88:91], %[vW], s[60:63], s68 offen offset:2048 sc1\n"
    "v_mfma_f32_16x16x32_bf16 a[100:103], v[56:59], v[100:103], a[100:103]\n"
    "v_mfma_f32_16x16x32_bf16 a[104:107], v[56:59], v[104:107], a[104:107]\n"
    "buffer_load_dwordx4 v[92:95], %[vW], s[60:63], s68 offen offset:3072 sc1\n"
    "v_mfma_f32_16x16x32_bf16 a[108:111], v[56:59], v[108:111], a[108:111]\n"
    "ds_read_b128 v[56:59], %[vA2] offset:31616\n"
    "v_mfma_f32_16x16x32_bf16 a[112:115], v[60:63], v[96:99], a[112:115]\n"
    "s_sub_u32 s74, s74, 1\n"
    "s_cselect_b32 s73, s77, s76\n"
    "v_mfma_f32_16x16x32_bf16 a[116:119], v[60:63], v[100:103], a[116:119]\n"
    "s_add_u32 s68, s68, s73\n"
    "v_mfma_f32_16x16x32_bf16 a[120:123], v[60:63], v[104:107], a[120:123]\n"
    "v_mfma_f32_16x16x32_bf16 a[124:127], v[60:63], v[108:111], a[124:127]\n"
    "ds_read_b128 v[60:63], %[vA2] offset:32640\n"
    "s_waitcnt vmcnt(14) lgkmcnt(6)\n"
    "v_mfma_f32_16x16x32_bf16 a[0:3], v[32:35], v[112:115], a[0:3]\n"
    "s_add_u32 s70, s69, 264192\n"
    "v_mfma_f32_16x16x32_bf16 a[4:7], v[32:35], v[116:119], a[4:7]\n"
    "s_add_u32 m0, %[mb], 23232\n"
    "v_mfma_f32_16x16x32_bf16 a[8:11], v[32:35], v[120:123], a[8:11]\n"
    "buffer_load_dwordx4 %[vW], s[64:67], s70 offen sc1 lds\n"
    "v_mfma_f32_16x16x32_bf16 a[12:15], v[32:35], v[124:127], a[12:15]\n"
    "ds_read_b128 v[32:35], %[vA0] offset:0\n"
    "v_mfma_f32_16x16x32_bf16 a[16:19], v[36:39], v[112:115], a[16:19]\n"
    "v_mfma_f32_16x16x32_bf16 a[20:23], v[36:39], v[116:119], a[20:23]\n"
    "buffer_load_dwordx4 v[96:99], %[vW], s[60:63], s68 offen offset:0 sc1\n"
    "v_mfma_f32_16x16x32_bf16 a[24:27], v[36:39], v[120:123], a[24:27]\n"
    "v_mfma_f32_16x16x32_bf16 a[28:31], v[36:39], v[124:127], a[28:31]\n"
    "ds_read_b128 v[36:39], %[vA0] offset:1024\n"
    "s_waitcnt lgkmcnt(6)\n"
    "v_mfma_f32_16x16x32_bf16 a[32:35], v[40:43], v[112:115], a[32:35]\n"
    "v_mfma_f32_16x16x32_bf16 a[36:39], v[40:43], v[116:119], a[36:39]\n"
    "v_mfma_f32_16x16x32_bf16 a[40:43], v[40:43], v[120:123], a[40:43]\n"
    "v_mfma_f32_16x16x32_bf16 a[44:47], v[40:43], v[124:127], a[44:47]\n"
    "ds_read_b128 v[40:43], %[vA0] offset:2048\n"
    "v_mfma_f32_16x16x32_bf16 a[48:51], v[44:47], v[112:115], a[48:51]\n"
    "v_mfma_f32_16x16x32_bf16 a[52:55], v[44:47], v[116:119], a[52:55]\n"
    "buffer_load_dwordx4 v[100:103], %[vW], s[60:63], s68 offen offset:1024 sc1\n"
    "v_mfma_f32_16x16x32_bf16 a[56:59], v[44:47], v[120:123], a[56:59]\n"
    "v_mfma_f32_16x16x32_bf16 a[60:63], v[44:47], v[124:127], a[60:63]\n"
    "ds_read_b128 v[44:47], %[vA0] offset:3072\n"
    "s_waitcnt lgkmcnt(6)\n"
    "v_mfma_f32_16x16x32_bf16 a[64:67], v[48:51], v[112:115], a[64:67]\n"
    "s_add_u32 s70, s69, 265216\n"
    "v_mfma_f32_16x16x32_bf16 a[68:71], v[48:51], v[116:119], a[68:71]\n"
    "s_add_u32 m0, %[mb], 24256\n"
    "v_mfma_f32_16x16x32_bf16 a[72:75], v[48:51], v[120:123], a[72:75]\n"
    "buffer_load_dwordx4 %[vW], s[64:67], s70 offen sc1 lds\n"
    "v_mfma_f32_16x16x32_bf16 a[76:79], v[48:51], v[124:127], a[76:79]\n"
    "ds_read_b128 v[48:51], %[vA0] offset:4224\n"
    "v_mfma_f32_16x16x32_bf16 a[80:83], v[52:55], v[112:115], a[80:83]\n"
    "s_sub_u32 s75, s75, 1\n"
    "s_cselect_b32 s79, s78, s76\n"
    "v_mfma_f32_16x16x32_bf16 a[84:87], v[52:55], v[116:119], a[84:87]\n"
    "buffer_load_dwordx4 v[104:107], %[vW], s[60:63], s68 offen offset:2048 sc1\n"
    "v_mfma_f32_16x16x32_bf16 a[88:91], v[52:55], v[120:123], a[88:91]\n"
    "s_add_u32 s69, s69, s79\n"
    "v_mfma_f32_16x16x32_bf16 a[92:95], v[52:55], v[124:127], a[92:95]\n"
    "ds_read_b128 v[52:55], %[vA0] offset:5248\n"
    "s_waitcnt lgkmcnt(6)\n"
    "v_mfma_f32_16x16x32_bf16 a[96:99], v[56:59], v[112:115], a[96:99]\n"
    "v_mfma_f32_16x16x32_bf16 a[100:103], v[56:59], v[116:119], a[100:103]\n"
    "v_mfma_f32_16x16x32_bf16 a[104:107], v[56:59], v[120:123], a[104:107]\n"
    "buffer_load_dwordx4 v[108:111], %[vW], s[60:63], s68 offen offset:3072 sc1\n"
    "v_mfma_f32_16x16x32_bf16 a[108:111], v[56:59], v[124:127], a[108:111]\n"
    "ds_read_b128 v[56:59], %[vA0] offset:6272\n"
    "v_mfma_f32_16x16x32_bf16 a[112:115], v[60:63], v[112:115], a[112:115]\n"
    "s_sub_u32 s74, s74, 1\n"
    "s_cselect_b32 s73, s77, s76\n"
    "v_mfma_f32_16x16x32_bf16 a[116:119], v[60:63], v[116:119], a[116:119]\n"
    "s_add_u32 s68, s68, s73\n"
    "v_mfma_f32_16x16x32_bf16 a[120:123], v[60:63], v[120:123], a[120:123]\n"
    "v_mfma_f32_16x16x32_bf16 a[124:127], v[60:63], v[124:127], a[124:127]\n"
    "ds_read_b128 v[60:63], %[vA0] offset:7296\n"
    "s_waitcnt vmcnt(13) lgkmcnt(6)\n"
    "v_mfma_f32_16x16x32_bf16 a[0:3], v[32:35], v[64:67], a[0:3]\n"
    "s_add_u32 s70, s69, 0\n"
    "v_mfma_f32_16x16x32_bf16 a[4:7], v[32:35], v[68:71], a[4:7]\n"
    "s_add_u32 m0, %[mb], 25408\n"
    "v_mfma_f32_16x16x32_bf16 a[8:11], v[32:35], v[72:75], a[8:11]\n"
    "buffer_load_dwordx4 %[vW], s[64:67], s70 offen sc1 lds\n"
    "v_mfma_f32_16x16x32_bf16 a[12:15], v[32:35], v[76:79], a[12:15]\n"
    "ds_read_b128 v[32:35], %[vA1] offset:0\n"
    "v_mfma_f32_16x16x32_bf16 a[16:19], v[36:39], v[64:67], a[16:19]\n"
    "v_mfma_f32_16x16x32_bf16 a[20:23], v[36:39], v[68:71], a[20:23]\n"
    "buffer_load_dwordx4 v[112:115], %[vW], s[60:63], s68 offen offset:0 sc1\n"
    "v_mfma_f32_16x16x32_bf16 a[24:27], v[36:39], v[72:75], a[24:27]\n"
    "v_mfma_f32_16x16x32_bf16 a[28:31], v[36:39], v[76:79], a[28:31]\n"
    "ds_read_b128 v[36:39], %[vA1] offset:1024\n"
    "s_waitcnt lgkmcnt(6)\n"
    "v_mfma_f32_16x16x32_bf16 a[32:35], v[40:43], v[64:67], a[32:35]\n"
    "v_mfma_f32_16x16x32_bf16 a[36:39], v[40:43], v[68:71], a[36:39]\n"
    "v_mfma_f32_16x16x32_bf16 a[40:43], v[40:43], v[72:75], a[40:43]\n"
    "s_add_u32 s70, s69, 1024\n"
    "v_mfma_f32_16x16x32_bf16 a[44:47], v[40:43], v[76:79], a[44:47]\n"
    "ds_read_b128 v[40:43], %[vA1] offset:2048\n"
    "v_mfma_f32_16x16x32_bf16 a[48:51], v[44:47], v[64:67], a[48:51]\n"
    "s_add_u32 m0, %[mb], 26432\n"
    "v_mfma_f32_16x16x32_bf16 a[52:55], v[44:47], v[68:71], a[52:55]\n"
    "buffer_load_dwordx4 %[vW], s[64:67], s70 offen sc1 lds\n"
    "v_mfma_f32_16x16x32_bf16 a[56:59], v[44:47], v[72:75], a[56:59]\n"
    "buffer_load_dwordx4 v[116:119], %[vW], s[60:63], s68 offen offset:1024 sc1\n"
    "v_mfma_f32_16x16x32_bf16 a[60:63], v[44:47], v[76:79], a[60:63]\n"
    "ds_read_b128 v[44:47], %[vA1] offset:3072\n"
    "s_waitcnt lgkmcnt(6)\n"
    "v_mfma_f32_16x16x32_bf16 a[64:67], v[48:51], v[64:67], a[64:67]\n"
    "v_mfma_f32_16x16x32_bf16 a[68:71], v[48:51], v[68:71], a[68:71]\n"
    "v_mfma_f32_16x16x32_bf16 a[72:75], v[48:51], v[72:75], a[72:75]\n"
    "v_mfma_f32_16x16x32_bf16 a[76:79], v[48:51], v[76:79], a[76:79]\n"
    "ds_read_b128 v[48:51], %[vA1] offset:4224\n"
    "v_mfma_f32_16x16x32_bf16 a[80:83], v[52:55], v[64:67], a[80:83]\n"
    "s_add_u32 s70, s69, 2048\n"
    "v_mfma_f32_16x16x32_bf16 a[84:87], v[52:55], v[68:71], a[84:87]\n"
    "s_add_u32 m0, %[mb], 27456\n"
    "v_mfma_f32_16x16x32_bf16 a[88:91], v[52:55], v[72:75], a[88:91]\n"
    "buffer_load_dwordx4 %[vW], s[64:67], s70 offen sc1 lds\n"
    "v_mfma_f32_16x16x32_bf16 a[92:95], v[52:55], v[76:79], a[92:95]\n"
    "ds_read_b128 v[52:55], %[vA1] offset:5248\n"
    "s_waitcnt lgkmcnt(6)\n"
    "v_mfma_f32_16x16x32_bf16 a[96:99], v[56:59], v[64:67], a[96:99]\n"
    "buffer_load_dwordx4 v[120:123], %[vW], s[60:63], s68 offen offset:2048 sc1\n"
    "v_mfma_f32_16x16x32_bf16 a[100:103], v[56:59], v[68:71], a[100:103]\n"
    "v_mfma_f32_16x16x32_bf16 a[104:107], v[56:59], v[72:75], a[104:107]\n"
    "buffer_load_dwordx4 v[124:127], %[vW], s[60:63], s68 offen offset:3072 sc1\n"
    "v_mfma_f32_16x16x32_bf16 a[108:111], v[56:59], v[76:79], a[108:111]\n"
    "ds_read_b128 v[56:59], %[vA1] offset:6272\n"
    "v_mfma_f32_16x16x32_bf16 a[112:115], v[60:63], v[64:67], a[112:115]\n"
    "s_sub_u32 s74, s74, 1\n"
    "s_cselect_b32 s73, s77, s76\n"
    "v_mfma_f32_16x16x32_bf16 a[116:119], v[60:63], v[68:71], a[116:119]\n"
    "s_add_u32 s68, s68, s73\n"
    "v_mfma_f32_16x16x32_bf16 a[120:123], v[60:63], v[72:75], a[120:123]\n"
    "v_mfma_f32_16x16x32_bf16 a[124:127], v[60:63], v[76:79], a[124:127]\n"
    "ds_read_b128 v[60:63], %[vA1] offset:7296\n"
    "s_waitcnt vmcnt(13) lgkmcnt(6)\n"
    "v_mfma_f32_16x16x32_bf16 a[0:3], v[32:35], v[80:83], a[0:3]\n"
    "s_add_u32 s70, s69, 3072\n"
    "v_mfma_f32_16x16x32_bf16 a[4:7], v[32:35], v[84:87], a[4:7]\n"
    "s_add_u32 m0, %[mb], 28480\n"
    "v_mfma_f32_16x16x32_bf16 a[8:11], v[32:35], v[88:91], a[8:11]\n"
    "buffer_load_dwordx4 %[vW], s[64:67], s70 offen sc1 lds\n"
    "v_mfma_f32_16x16x32_bf16 a[12:15], v[32:35], v[92:95], a[12:15]\n"
    "ds_read_b128 v[32:35], %[vA2] offset:0\n"
    "v_mfma_f32_16x16x32_bf16 a[16:19], v[36:39], v[80:83], a[16:19]\n"
    "v_mfma_f32_16x16x32_bf16 a[20:23], v[36:39], v[84:87], a[20:23]\n"
    "buffer_load_dwordx4 v[64:67], %[vW], s[60:63], s68 offen offset:0 sc1\n"
    "v_mfma_f32_16x16x32_bf16 a[24:27], v[36:39], v[88:91], a[24:27]\n"
    "v_mfma_f32_16x16x32_bf16 a[28:31], v[36:39], v[92:95], a[28:31]\n"
    "ds_read_b128 v[36:39], %[vA2] offset:1024\n"
    "s_waitcnt lgkmcnt(6)\n"
    "v_mfma_f32_16x16x32_bf16 a[32:35], v[40:43], v[80:83], a[32:35]\n"
    "v_mfma_f32_16x16x32_bf16 a[36:39], v[40:43], v[84:87], a[36:39]\n"
    "v_mfma_f32_16x16x32_bf16 a[40:43], v[40:43], v[88:91], a[40:43]\n"
    "s_add_u32 s70, s69, 262144\n"
    "v_mfma_f32_16x16x32_bf16 a[44:47], v[40:43], v[92:95], a[44:47]\n"
    "ds_read_b128 v[40:43], %[vA2] offset:2048\n"
    "v_mfma_f32_16x16x32_bf16 a[48:51], v[44:47], v[80:83], a[48:51]\n"
    "s_add_u32 m0, %[mb], 29632\n"
    "v_mfma_f32_16x16x32_bf16 a[52:55], v[44:47], v[84:87], a[52:55]\n"
    "buffer_load_dwordx4 %[vW], s[64:67], s70 offen sc1 lds\n"
    "v_mfma_f32_16x16x32_bf16 a[56:59], v[44:47], v[88:91], a[56:59]\n"
    "buffer_load_dwordx4 v[68:71], %[vW], s[60:63], s68 offen offset:1024 sc1\n"
    "v_mfma_f32_16x16x32_bf16 a[60:63], v[44:47], v[92:95], a[60:63]\n"
    "ds_read_b128 v[44:47], %[vA2] offset:3072\n"
    "s_waitcnt lgkmcnt(6)\n"
    "v_mfma_f32_16x16x32_bf16 a[64:67], v[48:51], v[80:83], a[64:67]\n"
    "v_mfma_f32_16x16x32_bf16 a[68:71], v[48:51], v[84:87], a[68:71]\n"
    "v_mfma_f32_16x16x32_bf16 a[72:75], v[48:51], v[88:91], a[72:75]\n"
    "v_mfma_f32_16x16x32_bf16 a[76:79], v[48:51], v[92:95], a[76:79]\n"
    "ds_read_b128 v[48:51], %[vA2] offset:4224\n"
    "v_mfma_f32_16x16x32_bf16 a[80:83], v[52:55], v[80:83], a[80:83]\n"
    "s_add_u32 s70, s69, 263168\n"
    "v_mfma_f32_16x16x32_bf16 a[84:87], v[52:55], v[84:87], a[84:87]\n"
    "s_add_u32 m0, %[mb], 30656\n"
    "v_mfma_f32_16x16x32_bf16 a[88:91], v[52:55], v[88:91], a[88:91]\n"
    "buffer_load_dwordx4 %[vW], s[64:67], s70 offen sc1 lds\n"
    "v_mfma_f32_16x16x32_bf16 a[92:95], v[52:55], v[92:95], a[92:95]\n"
    "ds_read_b128 v[52:55], %[vA2] offset:5248\n"
    "s_waitcnt lgkmcnt(6)\n"
    "v_mfma_f32_16x16x32_bf16 a[96:99], v[56:59], v[80:83], a[96:99]\n"
    "buffer_load_dwordx4 v[72:75], %[vW], s[60:63], s68 offen offset:2048 sc1\n"
    "v_mfma_f32_16x16x32_bf16 a[100:103], v[56:59], v[84:87], a[100:103]\n"
    "v_mfma_f32_16x16x32_bf16 a[104:107], v[56:59], v[88:91], a[104:107]\n"
    "buffer_load_dwordx4 v[76:79], %[vW], s[60:63], s68 offen offset:3072 sc1\n"
    "v_mfma_f32_16x16x32_bf16 a[108:111], v[56:59], v[92:95], a[108:111]\n"
    "ds_read_b128 v[56:59], %[vA2] offset:6272\n"
    "v_mfma_f32_16x16x32_bf16 a[112:115], v[60:63], v[80:83], a[112:115]\n"
    "s_sub_u32 s74, s74, 1\n"
    "s_cselect_b32 s73, s77, s76\n"
    "v_mfma_f32_16x16x32_bf16 a[116:119], v[60:63], v[84:87], a[116:119]\n"
    "s_add_u32 s68, s68, s73\n"
    "v_mfma_f32_16x16x32_bf16 a[120:123], v[60:63], v[88:91], a[120:123]\n"
    "v_mfma_f32_16x16x32_bf16 a[124:127], v[60:63], v[92:95], a[124:127]\n"
    "ds_read_b128 v[60:63], %[vA2] offset:7296\n"
    "s_waitcnt vmcnt(14) lgkmcnt(6)\n"
    "v_mfma_f32_16x16x32_bf16 a[0:3], v[32:35], v[96:99], a[0:3]\n"
    "s_add_u32 s70, s69, 264192\n"
    "v_mfma_f32_16x16x32_bf16 a[4:7], v[32:35], v[100:103], a[4:7]\n"
    "s_add_u32 m0, %[mb], 31680\n"
    "v_mfma_f32_16x16x32_bf16 a[8:11], v[32:35], v[104:107], a[8:11]\n"
    "buffer_load_dwordx4 %[vW], s[64:67], s70 offen sc1 lds\n"
    "v_mfma_f32_16x16x32_bf16 a[12:15], v[32:35], v[108:111], a[12:15]\n"
    "ds_read_b128 v[32:35], %[vA0] offset:8448\n"
    "v_mfma_f32_16x16x32_bf16 a[16:19], v[36:39], v[96:99], a[16:19]\n"
    "v_mfma_f32_16x16x32_bf16 a[20:23], v[36:39], v[100:103], a[20:23]\n"
    "buffer_load_dwordx4 v[80:83], %[vW], s[60:63], s68 offen offset:0 sc1\n"
    "v_mfma_f32_16x16x32_bf16 a[24:27], v[36:39], v[104:107], a[24:27]\n"
    "v_mfma_f32_16x16x32_bf16 a[28:31], v[36:39], v[108:111], a[28:31]\n"
    "ds_read_b128 v[36:39], %[vA0] offset:9472\n"
    "s_waitcnt lgkmcnt(6)\n"
    "v_mfma_f32_16x16x32_bf16 a[32:35], v[40:43], v[96:99], a[32:35]\n"
    "v_mfma_f32_16x16x32_bf16 a[36:39], v[40:43], v[100:103], a[36:39]\n"
    "v_mfma_f32_16x16x32_bf16 a[40:43], v[40:43], v[104:107], a[40:43]\n"
    "v_mfma_f32_16x16x32_bf16 a[44:47], v[40:43], v[108:111], a[44:47]\n"
    "ds_read_b128 v[40:43], %[vA0] offset:10496\n"
    "v_mfma_f32_16x16x32_bf16 a[48:51], v[44:47], v[96:99], a[48:51]\n"
    "v_mfma_f32_16x16x32_bf16 a[52:55], v[44:47], v[100:103], a[52:55]\n"
    "buffer_load_dwordx4 v[84:87], %[vW], s[60:63], s68 offen offset:1024 sc1\n"
    "v_mfma_f32_16x16x32_bf16 a[56:59], v[44:47], v[104:107], a[56:59]\n"
    "v_mfma_f32_16x16x32_bf16 a[60:63], v[44:47], v[108:111], a[60:63]\n"
    "ds_read_b128 v[44:47], %[vA0] offset:11520\n"
    "s_waitcnt lgkmcnt(6)\n"
    "v_mfma_f32_16x16x32_bf16 a[64:67], v[48:51], v[96:99], a[64:67]\n"
    "s_add_u32 s70, s69, 265216\n"
    "v_mfma_f32_16x16x32_bf16 a[68:71], v[48:51], v[100:103], a[68:71]\n"
    "s_add_u32 m0, %[mb], 32704\n"
    "v_mfma_f32_16x16x32_bf16 a[72:75], v[48:51], v[104:107], a[72:75]\n"
    "buffer_load_dwordx4 %[vW], s[64:67], s70 offen sc1 lds\n"
    "v_mfma_f32_16x16x32_bf16 a[76:79], v[48:51], v[108:111], a[76:79]\n"
    "ds_read_b128 v[48:51], %[vA0] offset:12672\n"
    "v_mfma_f32_16x16x32_bf16 a[80:83], v[52:55], v[96:99], a[80:83]\n"
    "s_sub_u32 s75, s75, 1\n"
    "s_cselect_b32 s79, s78, s76\n"
    "v_mfma_f32_16x16x32_bf16 a[84:87], v[52:55], v[100:103], a[84:87]\n"
    "buffer_load_dwordx4 v[88:91], %[vW], s[60:63], s68 offen offset:2048 sc1\n"
    "v_mfma_f32_16x16x32_bf16 a[88:91], v[52:55], v[104:107], a[88:91]\n"
    "s_add_u32 s69, s69, s79\n"
    "v_mfma_f32_16x16x32_bf16 a[92:95], v[52:55], v[108:111], a[92:95]\n"
    "ds_read_b128 v[52:55], %[vA0] offset:13696\n"
    "s_waitcnt lgkmcnt(6)\n"
    "v_mfma_f32_16x16x32_bf16 a[96:99], v[56:59], v[96:99], a[96:99]\n"
    "v_mfma_f32_16x16x32_bf16 a[100:103], v[56:59], v[100:103], a[100:103]\n"
    "v_mfma_f32_16x16x32_bf16 a[104:107], v[56:59], v[104:107], a[104:107]\n"
    "buffer_load_dwordx4 v[92:95], %[vW], s[60:63], s68 offen offset:3072 sc1\n"
    "v_mfma_f32_16x16x32_bf16 a[108:111], v[56:59], v[108:111], a[108:111]\n"
    "ds_read_b128 v[56:59], %[vA0] offset:14720\n"
    "v_mfma_f32_16x16x32_bf16 a[112:115], v[60:63], v[96:99], a[112:115]\n"
    "s_sub_u32 s74, s74, 1\n"
    "s_cselect_b32 s73, s77, s76\n"
    "v_mfma_f32_16x16x32_bf16 a[116:119], v[60:63], v[100:103], a[116:119]\n"
    "s_add_u32 s68, s68, s73\n"
    "v_mfma_f32_16x16x32_bf16 a[120:123], v[60:63], v[104:107], a[120:123]\n"
    "v_mfma_f32_16x16x32_bf16 a[124:127], v[60:63], v[108:111], a[124:127]\n"
    "ds_read_b128 v[60:63], %[vA0] offset:15744\n"
    "s_cmp_eq_u32 s71, 2\n"
    "s_cselect_b32 s66, 0, 327680\n"
    "s_waitcnt vmcnt(13) lgkmcnt(6)\n"
    "v_mfma_f32_16x16x32_bf16 a[0:3], v[32:35], v[112:115], a[0:3]\n"
    "s_add_u32 s70, s69, 0\n"
    "v_mfma_f32_16x16x32_bf16 a[4:7], v[32:35], v[116:119], a[4:7]\n"
    "s_add_u32 m0, %[mb], 64\n"
    "v_mfma_f32_16x16x32_bf16 a[8:11], v[32:35], v[120:123], a[8:11]\n"
    "buffer_load_dwordx4 %[vW], s[64:67], s70 offen sc1 lds\n"
    "v_mfma_f32_16x16x32_bf16 a[12:15], v[32:35], v[124:127], a[12:15]\n"
    "ds_read_b128 v[32:35], %[vA1] offset:8448\n"
    "v_mfma_f32_16x16x32_bf16 a[16:19], v[36:39], v[112:115], a[16:19]\n"
    "v_mfma_f32_16x16x32_bf16 a[20:23], v[36:39], v[116:119], a[20:23]\n"
    "buffer_load_dwordx4 v[96:99], %[vW], s[60:63], s68 offen offset:0 sc1\n"
    "v_mfma_f32_16x16x32_bf16 a[24:27], v[36:39], v[120:123], a[24:27]\n"
    "v_mfma_f32_16x16x32_bf16 a[28:31], v[36:39], v[124:127], a[28:31]\n"
    "ds_read_b128 v[36:39], %[vA1] offset:9472\n"
    "s_waitcnt lgkmcnt(6)\n"
    "v_mfma_f32_16x16x32_bf16 a[32:35], v[40:43], v[112:115], a[32:35]\n"
    "v_mfma_f32_16x16x32_bf16 a[36:39], v[40:43], v[116:119], a[36:39]\n"
    "v_mfma_f32_16x16x32_bf16 a[40:43], v[40:43], v[120:123], a[40:43]\n"
    "s_add_u32 s70, s69, 1024\n"
    "v_mfma_f32_16x16x32_bf16 a[44:47], v[40:43], v[124:127], a[44:47]\n"
    "ds_read_b128 v[40:43], %[vA1] offset:10496\n"
    "v_mfma_f32_16x16x32_bf16 a[48:51], v[44:47], v[112:115], a[48:51]\n"
    "s_add_u32 m0, %[mb], 1088\n"
    "v_mfma_f32_16x16x32_bf16 a[52:55], v[44:47], v[116:119], a[52:55]\n"
    "buffer_load_dwordx4 %[vW], s[64:67], s70 offen sc1 lds\n"
    "v_mfma_f32_16x16x32_bf16 a[56:59], v[44:47], v[120:123], a[56:59]\n"
    "buffer_load_dwordx4 v[100:103], %[vW], s[60:63], s68 offen offset:1024 sc1\n"
    "v_mfma_f32_16x16x32_bf16 a[60:63], v[44:47], v[124:127], a[60:63]\n"
    "ds_read_b128 v[44:47], %[vA1] offset:11520\n"
    "s_waitcnt lgkmcnt(6)\n"
    "v_mfma_f32_16x16x32_bf16 a[64:67], v[48:51], v[112:115], a[64:67]\n"
    "v_mfma_f32_16x16x32_bf16 a[68:71], v[48:51], v[116:119], a[68:71]\n"
    "v_mfma_f32_16x16x32_bf16 a[72:75], v[48:51], v[120:123], a[72:75]\n"
    "v_mfma_f32_16x16x32_bf16 a[76:79], v[48:51], v[124:127], a[76:79]\n"
    "ds_read_b128 v[48:51], %[vA1] offset:12672\n"
    "v_mfma_f32_16x16x32_bf16 a[80:83], v[52:55], v[112:115], a[80:83]\n"
    "s_add_u32 s70, s69, 2048\n"
    "v_mfma_f32_16x16x32_bf16 a[84:87], v[52:55], v[116:119], a[84:87]\n"
    "s_add_u32 m0, %[mb], 2112\n"
    "v_mfma_f32_16x16x32_bf16 a[88:91], v[52:55], v[120:123], a[88:91]\n"
    "buffer_load_dwordx4 %[vW], s[64:67], s70 offen sc1 lds\n"
    "v_mfma_f32_16x16x32_bf16 a[92:95], v[52:55], v[124:127], a[92:95]\n"
    "ds_read_b128 v[52:55], %[vA1] offset:13696\n"
    "s_waitcnt lgkmcnt(6)\n"
    "v_mfma_f32_16x16x32_bf16 a[96:99], v[56:59], v[112:115], a[96:99]\n"
    "buffer_load_dwordx4 v[104:107], %[vW], s[60:63], s68 offen offset:2048 sc1\n"
    "v_mfma_f32_16x16x32_bf16 a[100:103], v[56:59], v[116:119], a[100:103]\n"
    "v_mfma_f32_16x16x32_bf16 a[104:107], v[56:59], v[120:123], a[104:107]\n"
    "buffer_load_dwordx4 v[108:111], %[vW], s[60:63], s68 offen offset:3072 sc1\n"
    "v_mfma_f32_16x16x32_bf16 a[108:111], v[56:59], v[124:127], a[108:111]\n"
    "ds_read_b128 v[56:59], %[vA1] offset:14720\n"
    "v_mfma_f32_16x16x32_bf16 a[112:115], v[60:63], v[112:115], a[112:115]\n"
    "s_sub_u32 s74, s74, 1\n"
    "s_cselect_b32 s73, s77, s76\n"
    "v_mfma_f32_16x16x32_bf16 a[116:119], v[60:63], v[116:119], a[116:119]\n"
    "s_add_u32 s68, s68, s73\n"
    "v_mfma_f32_16x16x32_bf16 a[120:123], v[60:63], v[120:123], a[120:123]\n"
    "v_mfma_f32_16x16x32_bf16 a[124:127], v[60:63], v[124:127], a[124:127]\n"
    "ds_read_b128 v[60:63], %[vA1] offset:15744\n"
    "s_waitcnt vmcnt(13) lgkmcnt(6)\n"
    "v_mfma_f32_16x16x32_bf16 a[0:3], v[32:35], v[64:67], a[0:3]\n"
    "s_add_u32 s70, s69, 3072\n"
    "v_mfma_f32_16x16x32_bf16 a[4:7], v[32:35], v[68:71], a[4:7]\n"
    "s_add_u32 m0, %[mb], 3136\n"
    "v_mfma_f32_16x16x32_bf16 a[8:11], v[32:35], v[72:75], a[8:11]\n"
    "buffer_load_dwordx4 %[vW], s[64:67], s70 offen sc1 lds\n"
    "v_mfma_f32_16x16x32_bf16 a[12:15], v[32:35], v[76:79], a[12:15]\n"
    "ds_read_b128 v[32:35], %[vA2] offset:8448\n"
    "v_mfma_f32_16x16x32_bf16 a[16:19], v[36:39], v[64:67], a[16:19]\n"
    "v_mfma_f32_16x16x32_bf16 a[20:23], v[36:39], v[68:71], a[20:23]\n"
    "buffer_load_dwordx4 v[112:115], %[vW], s[60:63], s68 offen offset:0 sc1\n"
    "v_mfma_f32_16x16x32_bf16 a[24:27], v[36:39], v[72:75], a[24:27]\n"
    "v_mfma_f32_16x16x32_bf16 a[28:31], v[36:39], v[76:79], a[28:31]\n"
    "ds_read_b128 v[36:39], %[vA2] offset:9472\n"
    "s_waitcnt lgkmcnt(6)\n"
    "v_mfma_f32_16x16x32_bf16 a[32:35], v[40:43], v[64:67], a[32:35]\n"
    "v_mfma_f32_16x16x32_bf16 a[36:39], v[40:43], v[68:71], a[36:39]\n"
    "v_mfma_f32_16x16x32_bf16 a[40:43], v[40:43], v[72:75], a[40:43]\n"
    "s_add_u32 s70, s69, 262144\n"
    "v_mfma_f32_16x16x32_bf16 a[44:47], v[40:43], v[76:79], a[44:47]\n"
    "ds_read_b128 v[40:43], %[vA2] offset:10496\n"
    "v_mfma_f32_16x16x32_bf16 a[48:51], v[44:47], v[64:67], a[48:51]\n"
    "s_add_u32 m0, %[mb], 4288\n"
    "v_mfma_f32_16x16x32_bf16 a[52:55], v[44:47], v[68:71], a[52:55]\n"
    "buffer_load_dwordx4 %[vW], s[64:67], s70 offen sc1 lds\n"
    "v_mfma_f32_16x16x32_bf16 a[56:59], v[44:47], v[72:75], a[56:59]\n"
    "buffer_load_dwordx4 v[116:119], %[vW], s[60:63], s68 offen offset:1024 sc1\n"
    "v_mfma_f32_16x16x32_bf16 a[60:63], v[44:47], v[76:79], a[60:63]\n"
    "ds_read_b128 v[44:47], %[vA2] offset:11520\n"
    "s_waitcnt lgkmcnt(6)\n"
    "v_mfma_f32_16x16x32_bf16 a[64:67], v[48:51], v[64:67], a[64:67]\n"
    "v_mfma_f32_16x16x32_bf16 a[68:71], v[48:51], v[68:71], a[68:71]\n"
    "v_mfma_f32_16x16x32_bf16 a[72:75], v[48:51], v[72:75], a[72:75]\n"
    "v_mfma_f32_16x16x32_bf16 a[76:79], v[48:51], v[76:79], a[76:79]\n"
    "ds_read_b128 v[48:51], %[vA2] offset:12672\n"
    "v_mfma_f32_16x16x32_bf16 a[80:83], v[52:55], v[64:67], a[80:83]\n"
    "s_add_u32 s70, s69, 263168\n"
    "v_mfma_f32_16x16x32_bf16 a[84:87], v[52:55], v[68:71], a[84:87]\n"
    "s_add_u32 m0, %[mb], 5312\n"
    "v_mfma_f32_16x16x32_bf16 a[88:91], v[52:55], v[72:75], a[88:91]\n"
    "buffer_load_dwordx4 %[vW], s[64:67], s70 offen sc1 lds\n"
    "v_mfma_f32_16x16x32_bf16 a[92:95], v[52:55], v[76:79], a[92:95]\n"
    "ds_read_b128 v[52:55], %[vA2] offset:13696\n"
    "s_waitcnt lgkmcnt(6)\n"
    "v_mfma_f32_16x16x32_bf16 a[96:99], v[56:59], v[64:67], a[96:99]\n"
    "buffer_load_dwordx4 v[120:123], %[vW], s[60:63], s68 offen offset:2048 sc1\n"
    "v_mfma_f32_16x16x32_bf16 a[100:103], v[56:59], v[68:71], a[100:103]\n"
    "v_mfma_f32_16x16x32_bf16 a[104:107], v[56:59], v[72:75], a[104:107]\n"
    "buffer_load_dwordx4 v[124:127], %[vW], s[60:63], s68 offen offset:3072 sc1\n"
    "v_mfma_f32_16x16x32_bf16 a[108:111], v[56:59], v[76:79], a[108:111]\n"
    "ds_read_b128 v[56:59], %[vA2] offset:14720\n"
    "v_mfma_f32_16x16x32_bf16 a[112:115], v[60:63], v[64:67], a[112:115]\n"
    "s_sub_u32 s74, s74, 1\n"
    "s_cselect_b32 s73, s77, s76\n"
    "v_mfma_f32_16x16x32_bf16 a[116:119], v[60:63], v[68:71], a[116:119]\n"
    "s_add_u32 s68, s68, s73\n"
    "v_mfma_f32_16x16x32_bf16 a[120:123], v[60:63], v[72:75], a[120:123]\n"
    "v_mfma_f32_16x16x32_bf16 a[124:127], v[60:63], v[76:79], a[124:127]\n"
    "ds_read_b128 v[60:63], %[vA2] offset:15744\n"
    "s_waitcnt vmcnt(14) lgkmcnt(6)\n"
    "v_mfma_f32_16x16x32_bf16 a[0:3], v[32:35], v[80:83], a[0:3]\n"
    "s_add_u32 s70, s69, 264192\n"
    "v_mfma_f32_16x16x32_bf16 a[4:7], v[32:35], v[84:87], a[4:7]\n"
    "s_add_u32 m0, %[mb], 6336\n"
    "v_mfma_f32_16x16x32_bf16 a[8:11], v[32:35], v[88:91], a[8:11]\n"
    "buffer_load_dwordx4 %[vW], s[64:67], s70 offen sc1 lds\n"
    "v_mfma_f32_16x16x32_bf16 a[12:15], v[32:35], v[92:95], a[12:15]\n"
    "ds_read_b128 v[32:35], %[vA0] offset:16896\n"
    "v_mfma_f32_16x16x32_bf16 a[16:19], v[36:39], v[80:83], a[16:19]\n"
    "v_mfma_f32_16x16x32_bf16 a[20:23], v[36:39], v[84:87], a[20:23]\n"
    "buffer_load_dwordx4 v[64:67], %[vW], s[60:63], s68 offen offset:0 sc1\n"
    "v_mfma_f32_16x16x32_bf16 a[24:27], v[36:39], v[88:91], a[24:27]\n"
    "v_mfma_f32_16x16x32_bf16 a[28:31], v[36:39], v[92:95], a[28:31]\n"
    "ds_read_b128 v[36:39], %[vA0] offset:17920\n"
    "s_waitcnt lgkmcnt(6)\n"
    "v_mfma_f32_16x16x32_bf16 a[32:35], v[40:43], v[80:83], a[32:35]\n"
    "v_mfma_f32_16x16x32_bf16 a[36:39], v[40:43], v[84:87], a[36:39]\n"
    "v_mfma_f32_16x16x32_bf16 a[40:43], v[40:43], v[88:91], a[40:43]\n"
    "v_mfma_f32_16x16x32_bf16 a[44:47], v[40:43], v[92:95], a[44:47]\n"
    "ds_read_b128 v[40:43], %[vA0] offset:18944\n"
    "v_mfma_f32_16x16x32_bf16 a[48:51], v[44:47], v[80:83], a[48:51]\n"
    "v_mfma_f32_16x16x32_bf16 a[52:55], v[44:47], v[84:87], a[52:55]\n"
    "buffer_load_dwordx4 v[68:71], %[vW], s[60:63], s68 offen offset:1024 sc1\n"
    "v_mfma_f32_16x16x32_bf16 a[56:59], v[44:47], v[88:91], a[56:59]\n"
    "v_mfma_f32_16x16x32_bf16 a[60:63], v[44:47], v[92:95], a[60:63]\n"
    "ds_read_b128 v[44:47], %[vA0] offset:19968\n"
    "s_waitcnt lgkmcnt(6)\n"
    "v_mfma_f32_16x16x32_bf16 a[64:67], v[48:51], v[80:83], a[64:67]\n"
    "s_add_u32 s70, s69, 265216\n"
    "v_mfma_f32_16x16x32_bf16 a[68:71], v[48:51], v[84:87], a[68:71]\n"
    "s_add_u32 m0, %[mb], 7360\n"
    "v_mfma_f32_16x16x32_bf16 a[72:75], v[48:51], v[88:91], a[72:75]\n"
    "buffer_load_dwordx4 %[vW], s[64:67], s70 offen sc1 lds\n"
    "v_mfma_f32_16x16x32_bf16 a[76:79], v[48:51], v[92:95], a[76:79]\n"
    "ds_read_b128 v[48:51], %[vA0] offset:21120\n"
    "v_mfma_f32_16x16x32_bf16 a[80:83], v[52:55], v[80:83], a[80:83]\n"
    "s_sub_u32 s75, s75, 1\n"
    "s_cselect_b32 s79, s78, s76\n"
    "v_mfma_f32_16x16x32_bf16 a[84:87], v[52:55], v[84:87], a[84:87]\n"
    "buffer_load_dwordx4 v[72:75], %[vW], s[60:63], s68 offen offset:2048 sc1\n"
    "v_mfma_f32_16x16x32_bf16 a[88:91], v[52:55], v[88:91], a[88:91]\n"
    "s_add_u32 s69, s69, s79\n"
    "v_mfma_f32_16x16x32_bf16 a[92:95], v[52:55], v[92:95], a[92:95]\n"
    "ds_read_b128 v[52:55], %[vA0] offset:22144\n"
    "s_waitcnt lgkmcnt(6)\n"
    "v_mfma_f32_16x16x32_bf16 a[96:99], v[56:59], v[80:83], a[96:99]\n"
    "v_mfma_f32_16x16x32_bf16 a[100:103], v[56:59], v[84:87], a[100:103]\n"
    "v_mfma_f32_16x16x32_bf16 a[104:107], v[56:59], v[88:91], a[104:107]\n"
    "buffer_load_dwordx4 v[76:79], %[vW], s[60:63], s68 offen offset:3072 sc1\n"
    "v_mfma_f32_16x16x32_bf16 a[108:111], v[56:59], v[92:95], a[108:111]\n"
    "ds_read_b128 v[56:59], %[vA0] offset:23168\n"
    "v_mfma_f32_16x16x32_bf16 a[112:115], v[60:63], v[80:83], a[112:115]\n"
    "s_sub_u32 s74, s74, 1\n"
    "s_cselect_b32 s73, s77, s76\n"
    "v_mfma_f32_16x16x32_bf16 a[116:119], v[60:63], v[84:87], a[116:119]\n"
    "s_add_u32 s68, s68, s73\n"
    "v_mfma_f32_16x16x32_bf16 a[120:123], v[60:63], v[88:91], a[120:123]\n"
    "v_mfma_f32_16x16x32_bf16 a[124:127], v[60:63], v[92:95], a[124:127]\n"
    "ds_read_b128 v[60:63], %[vA0] offset:24192\n"
    "s_add_u32 s71, s71, 1\n"
    "s_cmp_lt_u32 s71, 3\n"
    "s_cbranch_scc1 LOOP%=\n"
    "s_waitcnt vmcnt(13) lgkmcnt(6)\n"
    "v_mfma_f32_16x16x32_bf16 a[0:3], v[32:35], v[96:99], a[0:3]\n"
    "v_mfma_f32_16x16x32_bf16 a[4:7], v[32:35], v[100:103], a[4:7]\n"
    "v_mfma_f32_16x16x32_bf16 a[8:11], v[32:35], v[104:107], a[8:11]\n"
    "v_mfma_f32_16x16x32_bf16 a[12:15], v[32:35], v[108:111], a[12:15]\n"
    "ds_read_b128 v[32:35], %[vA1] offset:16896\n"
    "v_mfma_f32_16x16x32_bf16 a[16:19], v[36:39], v[96:99], a[16:19]\n"
    "v_mfma_f32_16x16x32_bf16 a[20:23], v[36:39], v[100:103], a[20:23]\n"
    "buffer_load_dwordx4 v[80:83], %[vW], s[60:63], s68 offen offset:0 sc1\n"
    "v_mfma_f32_16x16x32_bf16 a[24:27], v[36:39], v[104:107], a[24:27]\n"
    "v_mfma_f32_16x16x32_bf16 a[28:31], v[36:39], v[108:111], a[28:31]\n"
    "ds_read_b128 v[36:39], %[vA1] offset:17920\n"
    "s_waitcnt lgkmcnt(6)\n"
    "v_mfma_f32_16x16x32_bf16 a[32:35], v[40:43], v[96:99], a[32:35]\n"
    "v_mfma_f32_16x16x32_bf16 a[36:39], v[40:43], v[100:103], a[36:39]\n"
    "v_mfma_f32_16x16x32_bf16 a[40:43], v[40:43], v[104:107], a[40:43]\n"
    "v_mfma_f32_16x16x32_bf16 a[44:47], v[40:43], v[108:111], a[44:47]\n"
    "ds_read_b128 v[40:43], %[vA1] offset:18944\n"
    "v_mfma_f32_16x16x32_bf16 a[48:51], v[44:47], v[96:99], a[48:51]\n"
    "v_mfma_f32_16x16x32_bf16 a[52:55], v[44:47], v[100:103], a[52:55]\n"
    "buffer_load_dwordx4 v[84:87], %[vW], s[60:63], s68 offen offset:1024 sc1\n"
    "v_mfma_f32_16x16x32_bf16 a[56:59], v[44:47], v[104:107], a[56:59]\n"
    "v_mfma_f32_16x16x32_bf16 a[60:63], v[44:47], v[108:111], a[60:63]\n"
    "ds_read_b128 v[44:47], %[vA1] offset:19968\n"
    "s_waitcnt lgkmcnt(6)\n"
    "v_mfma_f32_16x16x32_bf16 a[64:67], v[48:51], v[96:99], a[64:67]\n"
    "v_mfma_f32_16x16x32_bf16 a[68:71], v[48:51], v[100:103], a[68:71]\n"
    "v_mfma_f32_16x16x32_bf16 a[72:75], v[48:51], v[104:107], a[72:75]\n"
    "v_mfma_f32_16x16x32_bf16 a[76:79], v[48:51], v[108:111], a[76:79]\n"
    "ds_read_b128 v[48:51], %[vA1] offset:21120\n"
    "v_mfma_f32_16x16x32_bf16 a[80:83], v[52:55], v[96:99], a[80:83]\n"
    "v_mfma_f32_16x16x32_bf16 a[84:87], v[52:55], v[100:103], a[84:87]\n"
    "buffer_load_dwordx4 v[88:91], %[vW], s[60:63], s68 offen offset:2048 sc1\n"
    "v_mfma_f32_16x16x32_bf16 a[88:91], v[52:55], v[104:107], a[88:91]\n"
    "v_mfma_f32_16x16x32_bf16 a[92:95], v[52:55], v[108:111], a[92:95]\n"
    "ds_read_b128 v[52:55], %[vA1] offset:22144\n"
    "s_waitcnt lgkmcnt(6)\n"
    "v_mfma_f32_16x16x32_bf16 a[96:99], v[56:59], v[96:99], a[96:99]\n"
    "v_mfma_f32_16x16x32_bf16 a[100:103], v[56:59], v[100:103], a[100:103]\n"
    "v_mfma_f32_16x16x32_bf16 a[104:107], v[56:59], v[104:107], a[104:107]\n"
    "buffer_load_dwordx4 v[92:95], %[vW], s[60:63], s68 offen offset:3072 sc1\n"
    "v_mfma_f32_16x16x32_bf16 a[108:111], v[56:59], v[108:111], a[108:111]\n"
    "ds_read_b128 v[56:59], %[vA1] offset:23168\n"
    "v_mfma_f32_16x16x32_bf16 a[112:115], v[60:63], v[96:99], a[112:115]\n"
    "s_sub_u32 s74, s74, 1\n"
    "s_cselect_b32 s73, s77, s76\n"
    "v_mfma_f32_16x16x32_bf16 a[116:119], v[60:63], v[100:103], a[116:119]\n"
    "s_add_u32 s68, s68, s73\n"
    "v_mfma_f32_16x16x32_bf16 a[120:123], v[60:63], v[104:107], a[120:123]\n"
    "v_mfma_f32_16x16x32_bf16 a[124:127], v[60:63], v[108:111], a[124:127]\n"
    "ds_read_b128 v[60:63], %[vA1] offset:24192\n"
    "s_waitcnt vmcnt(10) lgkmcnt(6)\n"
    "v_mfma_f32_16x16x32_bf16 a[0:3], v[32:35], v[112:115], a[0:3]\n"
    "v_mfma_f32_16x16x32_bf16 a[4:7], v[32:35], v[116:119], a[4:7]\n"
    "v_mfma_f32_16x16x32_bf16 a[8:11], v[32:35], v[120:123], a[8:11]\n"
    "v_mfma_f32_16x16x32_bf16 a[12:15], v[32:35], v[124:127], a[12:15]\n"
    "ds_read_b128 v[32:35], %[vA2] offset:16896\n"
    "v_mfma_f32_16x16x32_bf16 a[16:19], v[36:39], v[112:115], a[16:19]\n"
    "v_mfma_f32_16x16x32_bf16 a[20:23], v[36:39], v[116:119], a[20:23]\n"
    "buffer_load_dwordx4 v[96:99], %[vW], s[60:63], s68 offen offset:0 sc1\n"
    "v_mfma_f32_16x16x32_bf16 a[24:27], v[36:39], v[120:123], a[24:27]\n"
    "v_mfma_f32_16x16x32_bf16 a[28:31], v[36:39], v[124:127], a[28:31]\n"
    "ds_read_b128 v[36:39], %[vA2] offset:17920\n"
    "s_waitcnt lgkmcnt(6)\n"
    "v_mfma_f32_16x16x32_bf16 a[32:35], v[40:43], v[112:115], a[32:35]\n"
    "v_mfma_f32_16x16x32_bf16 a[36:39], v[40:43], v[116:119], a[36:39]\n"
    "v_mfma_f32_16x16x32_bf16 a[40:43], v[40:43], v[120:123], a[40:43]\n"
    "v_mfma_f32_16x16x32_bf16 a[44:47], v[40:43], v[124:127], a[44:47]\n"
    "ds_read_b128 v[40:43], %[vA2] offset:18944\n"
    "v_mfma_f32_16x16x32_bf16 a[48:51], v[44:47], v[112:115], a[48:51]\n"
    "v_mfma_f32_16x16x32_bf16 a[52:55], v[44:47], v[116:119], a[52:55]\n"
    "buffer_load_dwordx4 v[100:103], %[vW], s[60:63], s68 offen offset:1024 sc1\n"
    "v_mfma_f32_16x16x32_bf16 a[56:59], v[44:47], v[120:123], a[56:59]\n"
    "v_mfma_f32_16x16x32_bf16 a[60:63], v[44:47], v[124:127], a[60:63]\n"
    "ds_read_b128 v[44:47], %[vA2] offset:19968\n"
    "s_waitcnt lgkmcnt(6)\n"
    "v_mfma_f32_16x16x32_bf16 a[64:67], v[48:51], v[112:115], a[64:67]\n"
    "v_mfma_f32_16x16x32_bf16 a[68:71], v[48:51], v[116:119], a[68:71]\n"
    "v_mfma_f32_16x16x32_bf16 a[72:75], v[48:51], v[120:123], a[72:75]\n"
    "v_mfma_f32_16x16x32_bf16 a[76:79], v[48:51], v[124:127], a[76:79]\n"
    "ds_read_b128 v[48:51], %[vA2] offset:21120\n"
    "v_mfma_f32_16x16x32_bf16 a[80:83], v[52:55], v[112:115], a[80:83]\n"
    "v_mfma_f32_16x16x32_bf16 a[84:87], v[52:55], v[116:119], a[84:87]\n"
    "buffer_load_dwordx4 v[104:107], %[vW], s[60:63], s68 offen offset:2048 sc1\n"
    "v_mfma_f32_16x16x32_bf16 a[88:91], v[52:55], v[120:123], a[88:91]\n"
    "v_mfma_f32_16x16x32_bf16 a[92:95], v[52:55], v[124:127], a[92:95]\n"
    "ds_read_b128 v[52:55], %[vA2] offset:22144\n"
    "s_waitcnt lgkmcnt(6)\n"
    "v_mfma_f32_16x16x32_bf16 a[96:99], v[56:59], v[112:115], a[96:99]\n"
    "v_mfma_f32_16x16x32_bf16 a[100:103], v[56:59], v[116:119], a[100:103]\n"
    "v_mfma_f32_16x16x32_bf16 a[104:107], v[56:59], v[120:123], a[104:107]\n"
    "buffer_load_dwordx4 v[108:111], %[vW], s[60:63], s68 offen offset:3072 sc1\n"
    "v_mfma_f32_16x16x32_bf16 a[108:111], v[56:59], v[124:127], a[108:111]\n"
    "ds_read_b128 v[56:59], %[vA2] offset:23168\n"
    "v_mfma_f32_16x16x32_bf16 a[112:115], v[60:63], v[112:115], a[112:115]\n"
    "s_sub_u32 s74, s74, 1\n"
    "s_cselect_b32 s73, s77, s76\n"
    "v_mfma_f32_16x16x32_bf16 a[116:119], v[60:63], v[116:119], a[116:119]\n"
    "s_add_u32 s68, s68, s73\n"
    "v_mfma_f32_16x16x32_bf16 a[120:123], v[60:63], v[120:123], a[120:123]\n"
    "v_mfma_f32_16x16x32_bf16 a[124:127], v[60:63], v[124:127], a[124:127]\n"
    "ds_read_b128 v[60:63], %[vA2] offset:24192\n"
    "s_waitcnt vmcnt(8) lgkmcnt(6)\n"
    "v_mfma_f32_16x16x32_bf16 a[0:3], v[32:35], v[64:67], a[0:3]\n"
    "v_mfma_f32_16x16x32_bf16 a[4:7], v[32:35], v[68:71], a[4:7]\n"
    "v_mfma_f32_16x16x32_bf16 a[8:11], v[32:35], v[72:75], a[8:11]\n"
    "v_mfma_f32_16x16x32_bf16 a[12:15], v[32:35], v[76:79], a[12:15]\n"
    "v_mfma_f32_16x16x32_bf16 a[16:19], v[36:39], v[64:67], a[16:19]\n"
    "v_mfma_f32_16x16x32_bf16 a[20:23], v[36:39], v[68:71], a[20:23]\n"
    "buffer_load_dwordx4 v[112:115], %[vW], s[60:63], s68 offen offset:0 sc1\n"
    "v_mfma_f32_16x16x32_bf16 a[24:27], v[36:39], v[72:75], a[24:27]\n"
    "v_mfma_f32_16x16x32_bf16 a[28:31], v[36:39], v[76:79], a[28:31]\n"
    "s_waitcnt lgkmcnt(4)\n"
    "v_mfma_f32_16x16x32_bf16 a[32:35], v[40:43], v[64:67], a[32:35]\n"
    "v_mfma_f32_16x16x32_bf16 a[36:39], v[40:43], v[68:71], a[36:39]\n"
    "v_mfma_f32_16x16x32_bf16 a[40:43], v[40:43], v[72:75], a[40:43]\n"
    "v_mfma_f32_16x16x32_bf16 a[44:47], v[40:43], v[76:79], a[44:47]\n"
    "v_mfma_f32_16x16x32_bf16 a[48:51], v[44:47], v[64:67], a[48:51]\n"
    "v_mfma_f32_16x16x32_bf16 a[52:55], v[44:47], v[68:71], a[52:55]\n"
    "buffer_load_dwordx4 v[116:119], %[vW], s[60:63], s68 offen offset:1024 sc1\n"
    "v_mfma_f32_16x16x32_bf16 a[56:59], v[44:47], v[72:75], a[56:59]\n"
    "v_mfma_f32_16x16x32_bf16 a[60:63], v[44:47], v[76:79], a[60:63]\n"
    "s_waitcnt lgkmcnt(2)\n"
    "v_mfma_f32_16x16x32_bf16 a[64:67], v[48:51], v[64:67], a[64:67]\n"
    "v_mfma_f32_16x16x32_bf16 a[68:71], v[48:51], v[68:71], a[68:71]\n"
    "v_mfma_f32_16x16x32_bf16 a[72:75], v[48:51], v[72:75], a[72:75]\n"
    "v_mfma_f32_16x16x32_bf16 a[76:79], v[48:51], v[76:79], a[76:79]\n"
    "v_mfma_f32_16x16x32_bf16 a[80:83], v[52:55], v[64:67], a[80:83]\n"
    "v_mfma_f32_16x16x32_bf16 a[84:87], v[52:55], v[68:71], a[84:87]\n"
    "buffer_load_dwordx4 v[120:123], %[vW], s[60:63], s68 offen offset:2048 sc1\n"
    "v_mfma_f32_16x16x32_bf16 a[88:91], v[52:55], v[72:75], a[88:91]\n"
    "v_mfma_f32_16x16x32_bf16 a[92:95], v[52:55], v[76:79], a[92:95]\n"
    "s_waitcnt lgkmcnt(0)\n"
    "v_mfma_f32_16x16x32_bf16 a[96:99], v[56:59], v[64:67], a[96:99]\n"
    "v_mfma_f32_16x16x32_bf16 a[100:103], v[56:59], v[68:71], a[100:103]\n"
    "v_mfma_f32_16x16x32_bf16 a[104:107], v[56:59], v[72:75], a[104:107]\n"
    "buffer_load_dwordx4 v[124:127], %[vW], s[60:63], s68 offen offset:3072 sc1\n"
    "v_mfma_f32_16x16x32_bf16 a[108:111], v[56:59], v[76:79], a[108:111]\n"
    "s_sub_u32 s74, s74, 1\n"
    "s_cselect_b32 s73, s77, s76\n"
    "v_mfma_f32_16x16x32_bf16 a[112:115], v[60:63], v[64:67], a[112:115]\n"
    "s_add_u32 s68, s68, s73\n"
    "v_mfma_f32_16x16x32_bf16 a[116:119], v[60:63], v[68:71], a[116:119]\n"
    "v_mfma_f32_16x16x32_bf16 a[120:123], v[60:63], v[72:75], a[120:123]\n"
    "v_mfma_f32_16x16x32_bf16 a[124:127], v[60:63], v[76:79], a[124:127]\n"
    "s_waitcnt vmcnt(34)\n"
    "ds_read_b128 v[32:35], %[vA0] offset:25344\n"
    "ds_read_b128 v[36:39], %[vA1] offset:25344\n"
    "ds_read_b128 v[40:43], %[vA2] offset:25344\n"
    "ds_read_b128 v[44:47], %[vA0] offset:26368\n"
    "ds_read_b128 v[48:51], %[vA1] offset:26368\n"
    "ds_read_b128 v[52:55], %[vA2] offset:26368\n"
    "ds_read_b128 v[56:59], %[vA0] offset:27392\n"
    "ds_read_b128 v[60:63], %[vA1] offset:27392\n"
    "s_waitcnt vmcnt(8) lgkmcnt(7)\n"
    "v_mfma_f32_16x16x32_bf16 a[0:3], v[32:35], v[80:83], a[0:3]\n"
    "v_mfma_f32_16x16x32_bf16 a[4:7], v[32:35], v[84:87], a[4:7]\n"
    "v_mfma_f32_16x16x32_bf16 a[8:11], v[32:35], v[88:91], a[8:11]\n"
    "v_mfma_f32_16x16x32_bf16 a[12:15], v[32:35], v[92:95], a[12:15]\n"
    "ds_read_b128 v[32:35], %[vA2] offset:27392\n"
    "s_waitcnt vmcnt(4) lgkmcnt(7)\n"
    "v_mfma_f32_16x16x32_bf16 a[0:3], v[36:39], v[96:99], a[0:3]\n"
    "v_mfma_f32_16x16x32_bf16 a[4:7], v[36:39], v[100:103], a[4:7]\n"
    "v_mfma_f32_16x16x32_bf16 a[8:11], v[36:39], v[104:107], a[8:11]\n"
    "v_mfma_f32_16x16x32_bf16 a[12:15], v[36:39], v[108:111], a[12:15]\n"
    "ds_read_b128 v[36:39], %[vA0] offset:28416\n"
    "s_waitcnt vmcnt(0) lgkmcnt(7)\n"
    "v_mfma_f32_16x16x32_bf16 a[0:3], v[40:43], v[112:115], a[0:3]\n"
    "v_mfma_f32_16x16x32_bf16 a[4:7], v[40:43], v[116:119], a[4:7]\n"
    "v_mfma_f32_16x16x32_bf16 a[8:11], v[40:43], v[120:123], a[8:11]\n"
    "v_mfma_f32_16x16x32_bf16 a[12:15], v[40:43], v[124:127], a[12:15]\n"
    "ds_read_b128 v[40:43], %[vA1] offset:28416\n"
    "s_waitcnt lgkmcnt(7)\n"
    "v_mfma_f32_16x16x32_bf16 a[16:19], v[44:47], v[80:83], a[16:19]\n"
    "v_mfma_f32_16x16x32_bf16 a[20:23], v[44:47], v[84:87], a[20:23]\n"
    "v_mfma_f32_16x16x32_bf16 a[24:27], v[44:47], v[88:91], a[24:27]\n"
    "v_mfma_f32_16x16x32_bf16 a[28:31], v[44:47], v[92:95], a[28:31]\n"
    "ds_read_b128 v[44:47], %[vA2] offset:28416\n"
    "s_waitcnt lgkmcnt(7)\n"
    "v_mfma_f32_16x16x32_bf16 a[16:19], v[48:51], v[96:99], a[16:19]\n"
    "v_mfma_f32_16x16x32_bf16 a[20:23], v[48:51], v[100:103], a[20:23]\n"
    "v_mfma_f32_16x16x32_bf16 a[24:27], v[48:51], v[104:107], a[24:27]\n"
    "v_mfma_f32_16x16x32_bf16 a[28:31], v[48:51], v[108:111], a[28:31]\n"
    "ds_read_b128 v[48:51], %[vA0] offset:29568\n"
    "s_waitcnt lgkmcnt(7)\n"
    "v_mfma_f32_16x16x32_bf16 a[16:19], v[52:55], v[112:115], a[16:19]\n"
    "ds_write_b128 %[vE], a[0:3] offset:0\n"
    "v_mfma_f32_16x16x32_bf16 a[20:23], v[52:55], v[116:119], a[20:23]\n"
    "ds_write_b128 %[vE], a[4:7] offset:1024\n"
    "v_mfma_f32_16x16x32_bf16 a[24:27], v[52:55], v[120:123], a[24:27]\n"
    "ds_write_b128 %[vE], a[8:11] offset:2048\n"
    "v_mfma_f32_16x16x32_bf16 a[28:31], v[52:55], v[124:127], a[28:31]\n"
    "ds_read_b128 v[52:55], %[vA1] offset:29568\n"
    "s_waitcnt lgkmcnt(10)\n"
    "v_mfma_f32_16x16x32_bf16 a[32:35], v[56:59], v[80:83], a[32:35]\n"
    "ds_write_b128 %[vE], a[12:15] offset:3072\n"
    "v_mfma_f32_16x16x32_bf16 a[36:39], v[56:59], v[84:87], a[36:39]\n"
    "v_mfma_f32_16x16x32_bf16 a[40:43], v[56:59], v[88:91], a[40:43]\n"
    "v_mfma_f32_16x16x32_bf16 a[44:47], v[56:59], v[92:95], a[44:47]\n"
    "ds_read_b128 v[56:59], %[vA2] offset:29568\n"
    "s_waitcnt lgkmcnt(11)\n"
    "v_mfma_f32_16x16x32_bf16 a[32:35], v[60:63], v[96:99], a[32:35]\n"
    "v_mfma_f32_16x16x32_bf16 a[36:39], v[60:63], v[100:103], a[36:39]\n"
    "v_mfma_f32_16x16x32_bf16 a[40:43], v[60:63], v[104:107], a[40:43]\n"
    "v_mfma_f32_16x16x32_bf16 a[44:47], v[60:63], v[108:111], a[44:47]\n"
    "ds_read_b128 v[60:63], %[vA0] offset:30592\n"
    "s_waitcnt lgkmcnt(11)\n"
    "v_mfma_f32_16x16x32_bf16 a[32:35], v[32:35], v[112:115], a[32:35]\n"
    "ds_write_b128 %[vE], a[16:19] offset:4160\n"
    "v_mfma_f32_16x16x32_bf16 a[36:39], v[32:35], v[116:119], a[36:39]\n"
    "ds_write_b128 %[vE], a[20:23] offset:5184\n"
    "v_mfma_f32_16x16x32_bf16 a[40:43], v[32:35], v[120:123], a[40:43]\n"
    "ds_write_b128 %[vE], a[24:27] offset:6208\n"
    "v_mfma_f32_16x16x32_bf16 a[44:47], v[32:35], v[124:127], a[44:47]\n"
    "ds_read_b128 v[32:35], %[vA1] offset:30592\n"
    "s_waitcnt lgkmcnt(14)\n"
    "v_mfma_f32_16x16x32_bf16 a[48:51], v[36:39], v[80:83], a[48:51]\n"
    "ds_write_b128 %[vE], a[28:31] offset:7232\n"
    "v_mfma_f32_16x16x32_bf16 a[52:55], v[36:39], v[84:87], a[52:55]\n"
    "v_mfma_f32_16x16x32_bf16 a[56:59], v[36:39], v[88:91], a[56:59]\n"
    "v_mfma_f32_16x16x32_bf16 a[60:63], v[36:39], v[92:95], a[60:63]\n"
    "ds_read_b128 v[36:39], %[vA2] offset:30592\n"
    "s_waitcnt lgkmcnt(15)\n"
    "v_mfma_f32_16x16x32_bf16 a[48:51], v[40:43], v[96:99], a[48:51]\n"
    "v_mfma_f32_16x16x32_bf16 a[52:55], v[40:43], v[100:103], a[52:55]\n"
    "v_mfma_f32_16x16x32_bf16 a[56:59], v[40:43], v[104:107], a[56:59]\n"
    "v_mfma_f32_16x16x32_bf16 a[60:63], v[40:43], v[108:111], a[60:63]\n"
    "ds_read_b128 v[40:43], %[vA0] offset:31616\n"
    "s_waitcnt lgkmcnt(15)\n"
    "v_mfma_f32_16x16x32_bf16 a[48:51], v[44:47], v[112:115], a[48:51]\n"
    "ds_write_b128 %[vE], a[32:35] offset:8320\n"
    "v_mfma_f32_16x16x32_bf16 a[52:55], v[44:47], v[116:119], a[52:55]\n"
    "ds_write_b128 %[vE], a[36:39] offset:9344\n"
    "v_mfma_f32_16x16x32_bf16 a[56:59], v[44:47], v[120:123], a[56:59]\n"
    "ds_write_b128 %[vE], a[40:43] offset:10368\n"
    "v_mfma_f32_16x16x32_bf16 a[60:63], v[44:47], v[124:127], a[60:63]\n"
    "ds_read_b128 v[44:47], %[vA1] offset:31616\n"
    "s_waitcnt lgkmcnt(15)\n"
    "v_mfma_f32_16x16x32_bf16 a[64:67], v[48:51], v[80:83], a[64:67]\n"
    "ds_write_b128 %[vE], a[44:47] offset:11392\n"
    "v_mfma_f32_16x16x32_bf16 a[68:71], v[48:51], v[84:87], a[68:71]\n"
    "v_mfma_f32_16x16x32_bf16 a[72:75], v[48:51], v[88:91], a[72:75]\n"
    "v_mfma_f32_16x16x32_bf16 a[76:79], v[48:51], v[92:95], a[76:79]\n"
    "ds_read_b128 v[48:51], %[vA2] offset:31616\n"
    "s_waitcnt lgkmcnt(15)\n"
    "v_mfma_f32_16x16x32_bf16 a[64:67], v[52:55], v[96:99], a[64:67]\n"
    "v_mfma_f32_16x16x32_bf16 a[68:71], v[52:55], v[100:103], a[68:71]\n"
    "v_mfma_f32_16x16x32_bf16 a[72:75], v[52:55], v[104:107], a[72:75]\n"
    "v_mfma_f32_16x16x32_bf16 a[76:79], v[52:55], v[108:111], a[76:79]\n"
    "ds_read_b128 v[52:55], %[vA0] offset:32640\n"
    "s_waitcnt lgkmcnt(15)\n"
    "v_mfma_f32_16x16x32_bf16 a[64:67], v[56:59], v[112:115], a[64:67]\n"
    "ds_write_b128 %[vE], a[48:51] offset:12480\n"
    "v_mfma_f32_16x16x32_bf16 a[68:71], v[56:59], v[116:119], a[68:71]\n"
    "ds_write_b128 %[vE], a[52:55] offset:13504\n"
    "v_mfma_f32_16x16x32_bf16 a[72:75], v[56:59], v[120:123], a[72:75]\n"
    "ds_write_b128 %[vE], a[56:59] offset:14528\n"
    "v_mfma_f32_16x16x32_bf16 a[76:79], v[56:59], v[124:127], a[76:79]\n"
    "ds_read_b128 v[56:59], %[vA1] offset:32640\n"
    "s_waitcnt lgkmcnt(15)\n"
    "v_mfma_f32_16x16x32_bf16 a[80:83], v[60:63], v[80:83], a[80:83]\n"
    "ds_write_b128 %[vE], a[60:63] offset:15552\n"
    "v_mfma_f32_16x16x32_bf16 a[84:87], v[60:63], v[84:87], a[84:87]\n"
    "v_mfma_f32_16x16x32_bf16 a[88:91], v[60:63], v[88:91], a[88:91]\n"
    "v_mfma_f32_16x16x32_bf16 a[92:95], v[60:63], v[92:95], a[92:95]\n"
    "ds_read_b128 v[60:63], %[vA2] offset:32640\n"
    "s_waitcnt lgkmcnt(15)\n"
    "v_mfma_f32_16x16x32_bf16 a[80:83], v[32:35], v[96:99], a[80:83]\n"
    "v_mfma_f32_16x16x32_bf16 a[84:87], v[32:35], v[100:103], a[84:87]\n"
    "v_mfma_f32_16x16x32_bf16 a[88:91], v[32:35], v[104:107], a[88:91]\n"
    "v_mfma_f32_16x16x32_bf16 a[92:95], v[32:35], v[108:111], a[92:95]\n"
    "s_waitcnt lgkmcnt(14)\n"
    "v_mfma_f32_16x16x32_bf16 a[80:83], v[36:39], v[112:115], a[80:83]\n"
    "ds_write_b128 %[vE], a[64:67] offset:16640\n"
    "v_mfma_f32_16x16x32_bf16 a[84:87], v[36:39], v[116:119], a[84:87]\n"
    "ds_write_b128 %[vE], a[68:71] offset:17664\n"
    "v_mfma_f32_16x16x32_bf16 a[88:91], v[36:39], v[120:123], a[88:91]\n"
    "ds_write_b128 %[vE], a[72:75] offset:18688\n"
    "v_mfma_f32_16x16x32_bf16 a[92:95], v[36:39], v[124:127], a[92:95]\n"
    "s_waitcnt lgkmcnt(15)\n"
    "v_mfma_f32_16x16x32_bf16 a[96:99], v[40:43], v[80:83], a[96:99]\n"
    "ds_write_b128 %[vE], a[76:79] offset:19712\n"
    "v_mfma_f32_16x16x32_bf16 a[100:103], v[40:43], v[84:87], a[100:103]\n"
    "v_mfma_f32_16x16x32_bf16 a[104:107], v[40:43], v[88:91], a[104:107]\n"
    "v_mfma_f32_16x16x32_bf16 a[108:111], v[40:43], v[92:95], a[108:111]\n"
    "s_waitcnt lgkmcnt(13)\n"
    "v_mfma_f32_16x16x32_bf16 a[96:99], v[44:47], v[96:99], a[96:99]\n"
    "v_mfma_f32_16x16x32_bf16 a[100:103], v[44:47], v[100:103], a[100:103]\n"
    "v_mfma_f32_16x16x32_bf16 a[104:107], v[44:47], v[104:107], a[104:107]\n"
    "v_mfma_f32_16x16x32_bf16 a[108:111], v[44:47], v[108:111], a[108:111]\n"
    "s_waitcnt lgkmcnt(11)\n"
    "v_mfma_f32_16x16x32_bf16 a[96:99], v[48:51], v[112:115], a[96:99]\n"
    "ds_write_b128 %[vE], a[80:83] offset:20800\n"
    "v_mfma_f32_16x16x32_bf16 a[100:103], v[48:51], v[116:119], a[100:103]\n"
    "ds_write_b128 %[vE], a[84:87] offset:21824\n"
    "v_mfma_f32_16x16x32_bf16 a[104:107], v[48:51], v[120:123], a[104:107]\n"
    "ds_write_b128 %[vE], a[88:91] offset:22848\n"
    "v_mfma_f32_16x16x32_bf16 a[108:111], v[48:51], v[124:127], a[108:111]\n"
    "s_waitcnt lgkmcnt(13)\n"
    "v_mfma_f32_16x16x32_bf16 a[112:115], v[52:55], v[80:83], a[112:115]\n"
    "ds_write_b128 %[vE], a[92:95] offset:23872\n"
    "v_mfma_f32_16x16x32_bf16 a[116:119], v[52:55], v[84:87], a[116:119]\n"
    "v_mfma_f32_16x16x32_bf16 a[120:123], v[52:55], v[88:91], a[120:123]\n"
    "v_mfma_f32_16x16x32_bf16 a[124:127], v[52:55], v[92:95], a[124:127]\n"
    "s_waitcnt lgkmcnt(10)\n"
    "v_mfma_f32_16x16x32_bf16 a[112:115], v[56:59], v[96:99], a[112:115]\n"
    "v_mfma_f32_16x16x32_bf16 a[116:119], v[56:59], v[100:103], a[116:119]\n"
    "v_mfma_f32_16x16x32_bf16 a[120:123], v[56:59], v[104:107], a[120:123]\n"
    "v_mfma_f32_16x16x32_bf16 a[124:127], v[56:59], v[108:111], a[124:127]\n"
    "s_waitcnt lgkmcnt(8)\n"
    "v_mfma_f32_16x16x32_bf16 a[112:115], v[60:63], v[112:115], a[112:115]\n"
    "ds_write_b128 %[vE], a[96:99] offset:24960\n"
    "v_mfma_f32_16x16x32_bf16 a[116:119], v[60:63], v[116:119], a[116:119]\n"
    "ds_write_b128 %[vE], a[100:103] offset:25984\n"
    "v_mfma_f32_16x16x32_bf16 a[120:123], v[60:63], v[120:123], a[120:123]\n"
    "ds_write_b128 %[vE], a[104:107] offset:27008\n"
    "v_mfma_f32_16x16x32_bf16 a[124:127], v[60:63], v[124:127], a[124:127]\n"
    "s_nop 7\n"
    "ds_write_b128 %[vE], a[108:111] offset:28032\n"
    "ds_write_b128 %[vE], a[112:115] offset:29120\n"
    "ds_write_b128 %[vE], a[116:119] offset:30144\n"
    "ds_write_b128 %[vE], a[120:123] offset:31168\n"
    "ds_write_b128 %[vE], a[124:127] offset:32192\n"
    "s_waitcnt vmcnt(0) lgkmcnt(0)\n"
        CONV_OPERANDS);
    }
}
DI void glds(unsigned ldsaddr, unsigned voff, const void* gp) {
    const unsigned m0v = __builtin_amdgcn_readfirstlane(ldsaddr);
    const size_t ga = (size_t)gp;
    const unsigned long long gs = ((unsigned long long)(unsigned)__builtin_amdgcn_readfirstlane((unsigned)(ga >> 32)) << 32) |
                                  (unsigned)__builtin_amdgcn_readfirstlane((unsigned)ga);
    asm volatile("s_nop 4\n\ts_mov_b32 m0, %0\n\ts_nop 0\n\tglobal_load_lds_dwordx4 %1, %2" :: "s"(m0v), "v"(voff), "s"(gs) : "memory");
}
DI void wload(char* lds, int ldsoff, const unsigned short* w, int pn, int nf, int wchunk, int half, int lane) {
    const char* wb = (const char*)w + ((size_t)(pn * 64 + nf * 16) * 512 + wchunk * 64 + half * 32) * 18;
    const unsigned ldsb = (unsigned)(size_t)(LAS char*)lds;
#pragma unroll
    for (int i = 0; i < 9; ++i) {
        const int sl = i * 64 + lane;
        const int r = (sl * 1821) >> 16;
        glds(ldsb + ldsoff + i * 1024, (unsigned)(r * 9216 + (sl - r * 36) * 16), wb);
    }
}
DI void wtransform(const char* raw, __amdgpu_buffer_rsrc_t rs, unsigned dst, int lane) {
    const char* src = raw + (lane & 15) * 576 + (lane >> 4) * 144;
    u32x4 rw[9], o12[12];
#pragma unroll
    for (int k = 0; k < 9; ++k) rw[k] = *(const u32x4*)(src + 16 * k);
    uprep(rw, o12);
#pragma unroll
    for (int xi = 0; xi < 4; ++xi)
#pragma unroll
        for (int dx = 0; dx < 3; ++dx) {
            st16(rs, dst + lane * 16 + (unsigned)((xi * 48 + dx) * 4096), o12[xi * 3 + dx]);
        }
}
DI void zero_halos(char* lds, int wv, int lane) {
    const int sl = lane >> 4, r = (lane >> 3) & 1, side = (lane >> 2) & 1, pc = lane & 3;
    *(u32x4*)(lds + wv * WREG + sl * SLOT + r * ROWP + (side ? 65 * 64 : 0) + pc * 16) = (u32x4){0, 0, 0, 0};
}
DI void first_chunks(char* lds, char* ws, unsigned voff, unsigned foff, float* red, int rdy, int b, int j, int n, int h, int lane, unsigned tag);
DI bool flags_ok(unsigned v, unsigned tag) { return __all(v == tag); }
DI void flags_wait(const char* ws, unsigned off, unsigned tag) {
    for (unsigned spins = 0;;) {
        if (flags_ok(ld4(ws, off), tag)) break;
        if (++spins > SPIN_CAP) break;
        __builtin_amdgcn_s_sleep(1);
    }
}

DI void first_chunks(char* lds, char* ws, unsigned voff, unsigned foff, float* red, int rdy, int b, int j, int n, int h, int lane, unsigned tag) {
    flags_wait(ws, foff + (unsigned)(((b * 16 + j) * 8 + (lane & 7)) * 128), tag);
    const unsigned ldsb = (unsigned)(size_t)(LAS char*)lds;
    const char* vb = ws + voff + ((((size_t)b * 32 + j * 2) * 4 + h) * 65536);
#pragma unroll
    for (int k = 2; k < 4; ++k) {
        const int c = (k - 2 < 2 * n) ? k - 2 : k;
#pragma unroll
        for (int i = 0; i < 8; ++i)
            asm volatile("s_nop 4\n\ts_mov_b32 m0, %0\n\ts_nop 0\n\tglobal_load_lds_dwordx4 %1, %2 sc1"
                         :: "s"(__builtin_amdgcn_readfirstlane(ldsb + h * WREG + k * SLOT + (i >> 2) * ROWP + 64 + (i & 3) * 1024)),
                            "v"((unsigned)(lane * 16 + (i >> 2) * 262144 + (i & 3) * 1024)), "s"(vb + c * 4096) : "memory");
    }
    drain();
    if (lane == 0) __hip_atomic_store((LAS unsigned*)(red + rdy + h), 1u, __ATOMIC_RELAXED, __HIP_MEMORY_SCOPE_WORKGROUP);
}

__global__ __launch_bounds__(512, 2) void resblock(const unsigned short* __restrict__ x, const unsigned short* __restrict__ temb,
                                                   const unsigned short* __restrict__ g1, const unsigned short* __restrict__ be1,
                                                   const unsigned short* __restrict__ w1, const unsigned short* __restrict__ wemb,
                                                   const unsigned short* __restrict__ g2, const unsigned short* __restrict__ be2,
                                                   const unsigned short* __restrict__ w2, char* __restrict__ ws,
                                                   unsigned short* __restrict__ out) {
    extern __shared__ __attribute__((aligned(16))) char lds[];
    const int t = threadIdx.x, lane = t & 63;
    const int wv = __builtin_amdgcn_readfirstlane(t >> 6);
    const int bid = blockIdx.x, xcd = bid & 7, idx = bid >> 3;
    const int ptile = (xcd >> 1) * 8 + (idx >> 2), n = (xcd & 1) * 4 + (idx & 3);
    const int b = ptile >> 4, j = ptile & 15, y0 = j * 4;
    const int grp = b * 4 + (n >> 1), slot = j * 2 + (n & 1);
    const __amdgpu_buffer_rsrc_t rs = __builtin_amdgcn_make_buffer_rsrc((void*)ws, 0, (int)WS_USED, 0x00020000);
    float* red = (float*)(lds + LDS_SCR);
    const int pn = bid >> 5, nf = (bid >> 3) & 3, wchunk = bid & 7;
    const unsigned ubase_s = (unsigned)((pn * 4 * 48 + (wchunk * 2 + (wv & 1)) * 3) * 4096 + nf * 1024);
    const unsigned fu_own = (unsigned)((pn * 32 + (bid & 31)) * 128 + (wv & 1) * 4);
    const float top = (j > 0) ? 1.f : 0.f, bot = (j < 15) ? 1.f : 0.f;
    unsigned tag;

    {
        const unsigned ldsb = (unsigned)(size_t)(LAS char*)lds;
        const int cc = wv & 1, xx = (wv >> 1) * 16 + (lane & 15), oq = lane >> 4;
        const int c8 = n * 64 + cc * 32 + oq * 8;
        if (t < 12) ((unsigned*)red)[240 + t] = 0u;
        const u32x4 gg = *(const u32x4*)(g1 + c8), bb = *(const u32x4*)(be1 + c8);
        SB();
        {
            const int ch = wv * 8 + (lane >> 3);
            const int fs = (((ch >> 1) & 1) | (((ch >> 3) & 1) << 1)) << 1;
            const unsigned voff = (unsigned)(ch * 8192 + (((lane & 7) ^ fs) << 4));
            const char* xb = (const char*)(x + ((size_t)b * 512 + n * 64) * 4096);
#pragma unroll
            for (int i = 0; i < 6; ++i) {
                const int r = i < 4 ? i + 1 : (i == 4 ? 0 : 5);
                int y = y0 - 1 + r; y = y < 0 ? 0 : (y > 63 ? 63 : y);
                glds(ldsb + (r >> 1) * WREG + (2 + (r & 1)) * SLOT + wv * 1024, voff, xb + y * 128);
            }
        }
        u32x4 we[6]; unsigned te[6];
        if (wv == 4 || wv == 5) wload(lds, (wv - 4) * WREG, w1, pn, nf, wchunk, wv & 1, lane);
        else if (wv == 1) {
            const unsigned short* wp = wemb + (size_t)(j * 48 + (lane >> 3)) * 512 + n * 64 + (lane & 7) * 8;
            const unsigned short* tp = temb + b * EE + j * 48 + (lane >> 3);
            asm volatile("global_load_dwordx4 %0, %12, off\n\tglobal_load_dwordx4 %1, %13, off\n\tglobal_load_dwordx4 %2, %14, off\n\t"
                         "global_load_dwordx4 %3, %15, off\n\tglobal_load_dwordx4 %4, %16, off\n\tglobal_load_dwordx4 %5, %17, off\n\t"
                         "global_load_ushort %6, %18, off\n\tglobal_load_ushort %7, %18, off offset:16\n\tglobal_load_ushort %8, %18, off offset:32\n\t"
                         "global_load_ushort %9, %18, off offset:48\n\tglobal_load_ushort %10, %18, off offset:64\n\tglobal_load_ushort %11, %18, off offset:80"
                         : "=&v"(we[0]), "=&v"(we[1]), "=&v"(we[2]), "=&v"(we[3]), "=&v"(we[4]), "=&v"(we[5]),
                           "=&v"(te[0]), "=&v"(te[1]), "=&v"(te[2]), "=&v"(te[3]), "=&v"(te[4]), "=&v"(te[5])
                         : "v"(wp), "v"(wp + 8 * 512), "v"(wp + 16 * 512), "v"(wp + 24 * 512), "v"(wp + 32 * 512), "v"(wp + 40 * 512), "v"(tp) : "memory");
        }
        SB();
        asm volatile("s_load_dword %0, %1, 0x0\n\ts_waitcnt lgkmcnt(0)\n\ts_add_u32 %0, %0, 1" : "=&s"(tag) : "s"(ws + OFF_G) : "memory", "scc");
        if (wv == 4 || wv == 5) asm volatile("s_waitcnt vmcnt(11)" ::: "memory");
        else if (wv == 1) asm volatile("s_waitcnt vmcnt(14)" ::: "memory");
        else asm volatile("s_waitcnt vmcnt(2)" ::: "memory");
        {
            f32x2 hs[16];
#pragma unroll
            for (int r = 1; r < 5; ++r) {
                const u32x4 d = *(const u32x4*)(lds + (r >> 1) * WREG + (2 + (r & 1)) * SLOT + wv * 1024 + lane * 16);
#pragma unroll
                for (int k = 0; k < 4; ++k) hs[(r - 1) * 4 + k] = bf2(d[k]);
            }
            wave_stats(hs, red, wv, lane);
        }
        unsigned xv[6][4];
        auto xread = [&](int r) {
            const int q = (lane >> 2) & 3, p = lane & 3, blk = wv >> 1;
#pragma unroll
            for (int hq = 0; hq < 2; ++hq) {
                const int ch = cc * 32 + oq * 8 + hq * 4 + q;
                const int fs = (((ch >> 1) & 1) | (((ch >> 3) & 1) << 1)) << 1;
                const int off = ch * 128 + (((blk * 2 + (p >> 1)) ^ fs) << 4) + (p & 1) * 8;
                const s16x4 d = __builtin_amdgcn_ds_read_tr16_b64_v4i16((LAS s16x4*)(lds + (r >> 1) * WREG + (2 + (r & 1)) * SLOT + off));
                const u32x2 u = __builtin_bit_cast(u32x2, d);
                xv[r][hq * 2] = u[0]; xv[r][hq * 2 + 1] = u[1];
            }
        };
        asm volatile("" :: "v"(gg), "v"(bb));
        if (wv == 4 || wv == 5) asm volatile("s_waitcnt vmcnt(9)" ::: "memory");
        else if (wv == 1) asm volatile("s_waitcnt vmcnt(12)" ::: "memory");
        else asm volatile("s_waitcnt vmcnt(0)" ::: "memory");
        lds_barrier();
        if (wv == 0) stats_publish(rs, OFF_S1, grp, slot, tag, red, lane);
        xread(1); xread(2); xread(3); xread(4); xread(0); xread(5);
        if (wv == 2) {
            red[96 + lane] = bflo(g2[n * 64 + lane]);
            red[160 + lane] = bflo(be2[n * 64 + lane]);
        }
        if (wv == 4 || wv == 5) {
            drain();
            wtransform(lds + (wv - 4) * WREG, rs, OFF_U1 + ubase_s, lane);
            drain();
            if (lane == 0) st4(ws, OFF_FU1 + fu_own, tag);
        } else if (wv == 1) {
            f32x2 ea[4] = {{0.f, 0.f}, {0.f, 0.f}, {0.f, 0.f}, {0.f, 0.f}};
            asm volatile("s_waitcnt vmcnt(0)" : "+v"(we[0]), "+v"(we[1]), "+v"(we[2]), "+v"(we[3]), "+v"(we[4]), "+v"(we[5]),
                         "+v"(te[0]), "+v"(te[1]), "+v"(te[2]), "+v"(te[3]), "+v"(te[4]), "+v"(te[5]) :: "memory");
            SB();
#pragma unroll
            for (int k = 0; k < 6; ++k) {
                const float sv = silu_f(bflo(te[k]));
#pragma unroll
                for (int e = 0; e < 4; ++e) ea[e] += sv * bf2(we[k][e]);
            }
            float ev[8];
#pragma unroll
            for (int e = 0; e < 4; ++e) { ev[2 * e] = ea[e][0]; ev[2 * e + 1] = ea[e][1]; }
#pragma unroll
            for (int e = 0; e < 8; ++e) {
                ev[e] += __shfl_xor(ev[e], 8, 64); ev[e] += __shfl_xor(ev[e], 16, 64); ev[e] += __shfl_xor(ev[e], 32, 64);
            }
            const int es = lane >> 3;
            float val = ev[0];
#pragma unroll
            for (int e = 1; e < 8; ++e) val = es == e ? ev[e] : val;
            const int chn = (lane & 7) * 8 + es;
            __hip_atomic_store((gu64*)(ws + OFF_EMB) + ((b * 8 + n) * 16 + j) * 64 + chn, ((unsigned long long)tag << 32) | __float_as_uint(val), RLX_AGENT);
        }
        if (wv == 0) stats_wait(rs, OFF_S1, grp, tag, red, lane);
        lds_barrier();
        if (wv < 4) zero_halos(lds, wv, lane);
        const float mean = red[16], rstd = red[17];
        const unsigned fuoff = OFF_FU1 + (unsigned)((n * 32 + (lane >> 1)) * 128 + (lane & 1) * 4);
        const unsigned fu_a = ld4(ws, fuoff);
        f32x2 sc[4], sh[4], sc2[4], sh2[4];
#pragma unroll
        for (int k = 0; k < 4; ++k) {
            sc[k] = bf2(gg[k]) * rstd; sh[k] = bf2(bb[k]) - mean * sc[k];
            sc2[k] = sc[k] * NLOG2E; sh2[k] = sh[k] * NLOG2E;
        }
        f32x2 a[6][4];
        auto arow = [&](int r) {
#pragma unroll
            for (int k = 0; k < 4; ++k) a[r][k] = silu2(bf2(xv[r][k]), sc[k], sh[k], sc2[k], sh2[k]);
        };
        const int pp = oq ^ ((((xx + 1) >> 2) & 1) << 1);
        const unsigned vg = OFF_V1 + (unsigned)((((b * 32 + j * 2) * 4) * 16 + n * 2 + cc) * 4096 + xx * 64 + pp * 16);
        char* vl = lds + cc * SLOT + (xx + 1) * 64 + pp * 16;
        auto vst = [&](int yl, int xi, const f32x2* pa, const f32x2* pb, float sg) {
            u32x4 d;
#pragma unroll
            for (int e = 0; e < 4; ++e) { const f32x2 u = pa[e] + sg * pb[e]; d[e] = pack2(u[0], u[1]); }
            st16(rs, vg + (unsigned)((yl * 4 + xi) * 65536), d);
            *(u32x4*)(vl + xi * WREG + yl * ROWP) = d;
        };
        arow(1); arow(2);
        SB();
        vst(0, 1, a[1], a[2], 1.f); vst(0, 2, a[2], a[1], -1.f);
        SB();
        arow(3);
        SB();
        vst(0, 3, a[1], a[3], -1.f);
        SB();
        arow(4);
        SB();
        vst(1, 0, a[2], a[4], -1.f); vst(1, 1, a[3], a[4], 1.f); vst(1, 2, a[4], a[3], -1.f);
        SB();
        const unsigned fu_b = ld4(ws, fuoff);
        arow(0); arow(5);
#pragma unroll
        for (int k = 0; k < 4; ++k) { a[0][k] *= top; a[5][k] *= bot; }
        SB();
        vst(0, 0, a[0], a[2], -1.f); vst(1, 3, a[3], a[5], -1.f);
        if (wv < 4 && !(flags_ok(fu_a, tag) || flags_ok(fu_b, tag))) flags_wait(ws, fuoff, tag);
    }

    {
        lds_barrier();
        if (wv < 4) {
            conv_main<true>(lds, ws, b, j, n, wv, fresh_tid() & 63, tag);
        } else {
            const int lane = fresh_tid() & 63;
            drain();
            if (last_arrival(red, 240, lane) && lane == 0) st4(ws, OFF_FV1 + (unsigned)((ptile * 8 + n) * 128), tag);
            first_chunks(lds, ws, OFF_V1, OFF_FV1, red, 244, b, j, n, wv - 4, lane, tag);
            if (wv >= 6) {
                for (unsigned spins = 0; __hip_atomic_load((LAS unsigned*)(red + 242 + 0), __ATOMIC_RELAXED, __HIP_MEMORY_SCOPE_WORKGROUP) == 0u;) {
                    if (++spins > (SPIN_CAP << 4)) break;
                    __builtin_amdgcn_s_sleep(4);
                }
                wload(lds, LDS_W + (wv & 1) * 9216, w2, pn, nf, wchunk, wv & 1, lane);
                drain();
                wtransform(lds + LDS_W + (wv & 1) * 9216, rs, OFF_U2 + ubase_s, lane);
            } else if (wv == 4) {
                __builtin_amdgcn_s_sleep(BGDELAY);
                gu64* eg = (gu64*)(ws + OFF_EMB) + (b * 8 + n) * 1024 + lane;
                unsigned long long g[16];
                for (unsigned spins = 0;;) {
                    bool ok = true;
#pragma unroll
                    for (int k = 0; k < 16; ++k) { g[k] = __hip_atomic_load(eg + k * 64, RLX_AGENT); }
#pragma unroll
                    for (int k = 0; k < 16; ++k) ok = ok && (unsigned)(g[k] >> 32) == tag;
                    if (__all(ok)) break;
                    if (++spins > SPIN_CAP) break;
                    __builtin_amdgcn_s_sleep(32);
                }
                float e = 0.f;
#pragma unroll
                for (int k = 0; k < 16; ++k) e += __uint_as_float((unsigned)g[k]);
                red[32 + lane] = e;
            }
        }
        const int t = fresh_tid(), lane = t & 63;
        const int oc = t & 7, xx = t >> 3;
        lds_barrier();
        f32x2 em[4], gg[4], bb[4];
#pragma unroll
        for (int k = 0; k < 4; ++k) {
            em[k] = *(const f32x2*)(red + 32 + oc * 8 + 2 * k); gg[k] = *(const f32x2*)(red + 96 + oc * 8 + 2 * k); bb[k] = *(const f32x2*)(red + 160 + oc * 8 + 2 * k);
        }
        f32x2 h[16];
#pragma unroll
        for (int yl = 0; yl < 2; ++yl) {
            const char* p0 = lds + (yl * 64 + xx) * E1_PITCH + oc * 32;
            f32x4 m[4][2];
#pragma unroll
            for (int xi = 0; xi < 4; ++xi) { m[xi][0] = *(const f32x4*)(p0 + xi * WREG); m[xi][1] = *(const f32x4*)(p0 + xi * WREG + 16); }
#pragma unroll
            for (int k = 0; k < 4; ++k) {
                const int hf = k >> 1, e = (k & 1) * 2;
                const f32x2 m0 = {m[0][hf][e], m[0][hf][e + 1]}, m1 = {m[1][hf][e], m[1][hf][e + 1]};
                const f32x2 m2 = {m[2][hf][e], m[2][hf][e + 1]}, m3 = {m[3][hf][e], m[3][hf][e + 1]};
                const f32x2 t1 = m1 + em[k];
                h[(2 * yl) * 4 + k] = (m0 + m2) + t1;
                h[(2 * yl + 1) * 4 + k] = (t1 - m2) - m3;
            }
        }
        wave_stats(h, red, wv, lane);
        lds_barrier();
        if (wv == 0) stats_publish(rs, OFF_S2, grp, slot, tag, red, lane);
#pragma unroll
        for (int side = 0; side < 2; ++side) {
            if (side == 0 ? (j > 0) : (j < 15)) {
                u32x4 d;
#pragma unroll
                for (int k = 0; k < 4; ++k) d[k] = pack2(h[side * 12 + k][0], h[side * 12 + k][1]);
                st16(rs, OFF_HX + (unsigned)(((ptile * 8 + n) * 2 + side) * 8192 + xx * 128 + oc * 16), d);
            }
        }
        if (wv >= 4) zero_halos(lds, wv - 4, lane);
        const unsigned fu2off = OFF_FU2 + (unsigned)((n * 32 + (lane >> 1)) * 128 + (lane & 1) * 4);
        const int pup = j > 0 ? ptile - 1 : ptile, pdn = j < 15 ? ptile + 1 : ptile;
        const unsigned fhoff = OFF_FH + (unsigned)((((lane & 1) ? pdn : pup) * 8 + n) * 128);
        drain();
        if (wv >= 6 && lane == 0) st4(ws, OFF_FU2 + fu_own, tag);
        if (last_arrival(red, 243, lane) && lane == 0) st4(ws, OFF_FH + (unsigned)((ptile * 8 + n) * 128), tag);
        u32x4 hx[2];
        auto halo_loads = [&]() {
#pragma unroll
            for (int side = 0; side < 2; ++side)
                hx[side] = ld16(rs, OFF_HX + (unsigned)((((side ? pdn : pup) * 8 + n) * 2 + (1 - side)) * 8192 + xx * 128 + oc * 16));
        };
        if (wv == 0) stats_wait(rs, OFF_S2, grp, tag, red, lane);
        const unsigned fh0 = wv == 0 ? ld4(ws, fhoff) : 0u;
        bool halo_early = false;
        if (wv != 0) { flags_wait(ws, fhoff, tag); halo_loads(); halo_early = true; }
        const unsigned fu2 = ld4(ws, fu2off);
        lds_barrier();
        if (!halo_early) { if (!flags_ok(fh0, tag)) flags_wait(ws, fhoff, tag); halo_loads(); }
        const float mean = red[16], rstd = red[17];
        f32x2 sc[4], sh[4], sc2[4], sh2[4];
#pragma unroll
        for (int k = 0; k < 4; ++k) {
            sc[k] = gg[k] * rstd; sh[k] = bb[k] - mean * sc[k];
            sc2[k] = sc[k] * NLOG2E; sh2[k] = sh[k] * NLOG2E;
        }
        const int pp = (oc & 3) ^ ((((xx + 1) >> 2) & 1) << 1);
        const unsigned vg = OFF_V2 + (unsigned)(((b * 32 + j * 2) * 4 * 16 + n * 2 + (oc >> 2)) * 4096 + xx * 64 + pp * 16);
        char* vl = lds + (oc >> 2) * SLOT + (xx + 1) * 64 + pp * 16;
        auto vst = [&](int yl, int xi, const f32x2* pa, const f32x2* pb, float sg) {
            u32x4 d;
#pragma unroll
            for (int e = 0; e < 4; ++e) { const f32x2 u = pa[e] + sg * pb[e]; d[e] = pack2(u[0], u[1]); }
            st16(rs, vg + (unsigned)((yl * 4 + xi) * 65536), d);
            *(u32x4*)(vl + xi * WREG + yl * ROWP) = d;
        };
        f32x2 a[6][4];
        auto arow = [&](int r) {
#pragma unroll
            for (int k = 0; k < 4; ++k) a[r + 1][k] = silu2(h[r * 4 + k], sc[k], sh[k], sc2[k], sh2[k]);
        };
        arow(0); arow(1);
        SB();
        vst(0, 1, a[1], a[2], 1.f); vst(0, 2, a[2], a[1], -1.f);
        SB();
        arow(2);
        SB();
        vst(0, 3, a[1], a[3], -1.f);
        SB();
        arow(3);
        SB();
        vst(1, 0, a[2], a[4], -1.f); vst(1, 1, a[3], a[4], 1.f); vst(1, 2, a[4], a[3], -1.f);
        SB();
        const unsigned fu2b = ld4(ws, fu2off);
#pragma unroll
        for (int k = 0; k < 4; ++k) {
            a[0][k] = top * silu2(bf2(hx[0][k]), sc[k], sh[k], sc2[k], sh2[k]);
            a[5][k] = bot * silu2(bf2(hx[1][k]), sc[k], sh[k], sc2[k], sh2[k]);
        }
        vst(0, 0, a[0], a[2], -1.f); vst(1, 3, a[3], a[5], -1.f);
        if (wv < 4 && !(flags_ok(fu2, tag) || flags_ok(fu2b, tag))) flags_wait(ws, fu2off, tag);
    }

    {
        u32x4 xr[2][2];
        {
            const int t = fresh_tid();
            const int oct = t & 7, yl = (t >> 3) & 1, cr = t >> 4;
#pragma unroll
            for (int i = 0; i < 2; ++i)
#pragma unroll
                for (int r = 0; r < 2; ++r)
                    xr[i][r] = *(const u32x4*)(x + (((size_t)b * 512 + n * 64 + cr + 32 * i) * 64 + y0 + 2 * yl + r) * 64 + oct * 8);
        }
        lds_barrier();
        if (wv < 4) {
            conv_main<false>(lds, ws, b, j, n, wv, fresh_tid() & 63, tag);
        } else {
            const int lane = fresh_tid() & 63;
            drain();
            if (last_arrival(red, 241, lane) && lane == 0) st4(ws, OFF_FV2 + (unsigned)((ptile * 8 + n) * 128), tag);
            first_chunks(lds, ws, OFF_V2, OFF_FV2, red, 248, b, j, n, wv - 4, lane, tag);
            if (bid == 0) {
#pragma unroll
                for (int k = 0; k < 1; ++k) {
                    if (wv == 4) {
                        for (int q4 = 0; q4 < 4; ++q4) flags_wait(ws, OFF_FV1 + (unsigned)((q4 * 64 + lane) * 128), tag);
                        st4(ws, OFF_G, tag);
                    }
                }
            }
        }
        lds_barrier();
        const int t = fresh_tid(), lane = t & 63;
        const int oct = t & 7, yl = (t >> 3) & 1, cr = t >> 4;
        const __amdgpu_buffer_rsrc_t ro = __builtin_amdgcn_make_buffer_rsrc((void*)out, 0, 2 * 512 * 4096 * 2, 0x00020000);
#pragma unroll
        for (int i = 0; i < 2; ++i) {
            const int ch = cr + 32 * i;
            const char* p0 = lds + (yl * 4 + (oct >> 1)) * E2_BLK + ch * 64;
            const int s0 = (((oct & 1) * 2) ^ ((ch >> 1) & 3)) << 4, s1 = (((oct & 1) * 2 + 1) ^ ((ch >> 1) & 3)) << 4;
            f32x4 m[4][2];
#pragma unroll
            for (int xi = 0; xi < 4; ++xi) { m[xi][0] = *(const f32x4*)(p0 + xi * WREG + s0); m[xi][1] = *(const f32x4*)(p0 + xi * WREG + s1); }
            const unsigned go = (unsigned)(((((b * 512 + n * 64 + ch) * 64 + y0 + 2 * yl) * 64) + oct * 8) * 2);
            u32x4 d0, d1;
#pragma unroll
            for (int e = 0; e < 4; ++e) {
                const int hf = e >> 1, k = (e & 1) * 2;
                d0[e] = pack2(m[0][hf][k] + m[1][hf][k] + m[2][hf][k] + bflo(xr[i][0][e]), m[0][hf][k + 1] + m[1][hf][k + 1] + m[2][hf][k + 1] + bfhi(xr[i][0][e]));
                d1[e] = pack2(m[1][hf][k] - m[2][hf][k] - m[3][hf][k] + bflo(xr[i][1][e]), m[1][hf][k + 1] - m[2][hf][k + 1] - m[3][hf][k + 1] + bfhi(xr[i][1][e]));
            }
            st16(ro, go, d0);
            st16(ro, go + 128, d1);
        }
    }
}

extern "C" void kernel_launch(void* const* d_in, const int* in_sizes, int n_in, void* d_out, int out_size, void* d_ws,
                              size_t ws_size, hipStream_t stream) {
    (void)hipFuncSetAttribute((const void*)resblock, hipFuncAttributeMaxDynamicSharedMemorySize, LDS_BYTES);
    resblock<<<256, 512, LDS_BYTES, stream>>>((const unsigned short*)d_in[0], (const unsigned short*)d_in[1], (const unsigned short*)d_in[2],
                                              (const unsigned short*)d_in[3], (const unsigned short*)d_in[4], (const unsigned short*)d_in[5],
                                              (const unsigned short*)d_in[6], (const unsigned short*)d_in[7], (const unsigned short*)d_in[8],
                                              (char*)d_ws, (unsigned short*)d_out);
}
```
